```python
import torch

def _softmax(x, axis=-1):
    return torch.softmax(x, dim=axis)
def _gelu(x):
    return torch.nn.functional.gelu(x, approximate="tanh")
def _silu(x):
    return torch.nn.functional.silu(x)
def _sigmoid(x):
    return torch.sigmoid(x)
def _layernorm(x, axis=-1, eps=1e-5):
    m = x.mean(dim=axis, keepdim=True)
    v = x.var(dim=axis, keepdim=True, unbiased=False)
    return (x - m) / torch.sqrt(v + eps)
def _scatter_add(tgt, idx, src, axis=0):
    return tgt.clone().index_add_(axis, idx, src)
def _segment_sum(src, seg, n):
    out = torch.zeros((n,) + src.shape[1:], dtype=src.dtype)
    return out.index_add_(0, seg, src)
def _pad(x, pw):
    flat = tuple(v for pair in reversed(pw) for v in pair)
    return torch.nn.functional.pad(x, flat)

kernel_name = 'gated_mix__mha_block_cached__gqa_block_cached__decode'

def setup_inputs():

    _w = (lambda a: (lambda t: t.to(torch.bfloat16) if t.is_floating_point() else t)(torch.as_tensor(a)))
    def _A_setup():
        B, Sq, D = 8, 1, 1024
        H, Sk, Dh = 32, 4096, 128
        return {
            "x": _w((lambda *s: torch.randn(*s, dtype=torch.bfloat16) * 0.05)(B, Sq, D)),
            "k_cache": _w((lambda *s: torch.randn(*s, dtype=torch.bfloat16) * 6.4)(B, H, Sk, Dh)),
            "v_cache": _w((lambda *s: torch.randn(*s, dtype=torch.bfloat16) * 0.05)(B, H, Sk, Dh)),
            "wq": _w((lambda *s: torch.randn(*s, dtype=torch.bfloat16) * 6.4)(D, H * Dh) * (D ** -0.5)),
            "wo": _w((lambda *s: torch.randn(*s, dtype=torch.bfloat16) * 0.05)(H * Dh, D) * ((H * Dh) ** -0.5)),
        }
    _A_in = _A_setup()

    def _B_setup():
        B, Sq, D = 8, 1, 1024
        Hq, Hkv, Sk, Dh = 32, 4, 4096, 128
        assert Hq % Hkv == 0, (Hq, Hkv)
        return {
            "x": _w((lambda *s: torch.randn(*s, dtype=torch.bfloat16) * 0.05)(B, Sq, D)),
            "k_cache": _w((lambda *s: torch.randn(*s, dtype=torch.bfloat16) * 6.4)(B, Hkv, Sk, Dh)),
            "v_cache": _w((lambda *s: torch.randn(*s, dtype=torch.bfloat16) * 0.05)(B, Hkv, Sk, Dh)),
            "wq": _w((lambda *s: torch.randn(*s, dtype=torch.bfloat16) * 6.4)(D, Hq * Dh) * (D ** -0.5)),
            "wo": _w((lambda *s: torch.randn(*s, dtype=torch.bfloat16) * 0.05)(Hq * Dh, D) * ((Hq * Dh) ** -0.5)),
        }
    _B_in = _B_setup()

    return {
        "A_x": _A_in["x"],
        "A_k_cache": _A_in["k_cache"],
        "A_v_cache": _A_in["v_cache"],
        "A_wq": _A_in["wq"],
        "A_wo": _A_in["wo"],
        "B_x": _B_in["x"],
        "B_k_cache": _B_in["k_cache"],
        "B_v_cache": _B_in["v_cache"],
        "B_wq": _B_in["wq"],
        "B_wo": _B_in["wo"],
        "gate_w": _w((lambda *s: torch.randn(*s, dtype=torch.bfloat16) * 0.05)(1024) * 16.0),
    }

def reference(A_x, A_k_cache, A_v_cache, A_wq, A_wo, B_x, B_k_cache, B_v_cache, B_wq, B_wo, gate_w):
    def _A_ref(x, k_cache, v_cache, wq, wo):
        B, Sq, D = x.shape
        H, Dh = 32, 128
        q = (lambda x, *d: x.permute(*d))((x @ wq).reshape(B, Sq, H, Dh), 0, 2, 1, 3)
        scores = torch.einsum("bhqd,bhkd->bhqk", q, k_cache) * (Dh ** -0.5)
        p = _softmax(scores, axis=-1)
        o = torch.einsum("bhqk,bhkd->bhqd", p, v_cache)
        return (lambda x, *d: x.permute(*d))(o, 0, 2, 1, 3).reshape(B, Sq, H * Dh) @ wo

    def _B_ref(x, k_cache, v_cache, wq, wo):
        B, Sq, D = x.shape
        Hq, Dh = 32, 128
        Hkv = k_cache.shape[1]
        rep = Hq // Hkv
        q = (lambda x, *d: x.permute(*d))((x @ wq).reshape(B, Sq, Hq, Dh), 0, 2, 1, 3)
        k = (lambda x, n, axis: torch.repeat_interleave(x, n, dim=axis))(k_cache, rep, axis=1)
        v = (lambda x, n, axis: torch.repeat_interleave(x, n, dim=axis))(v_cache, rep, axis=1)
        scores = torch.einsum("bhqd,bhkd->bhqk", q, k) * (Dh ** -0.5)
        p = _softmax(scores, axis=-1)
        o = torch.einsum("bhqk,bhkd->bhqd", p, v)
        return (lambda x, *d: x.permute(*d))(o, 0, 2, 1, 3).reshape(B, Sq, Hq * Dh) @ wo

    x = A_x
    g = _sigmoid((x * gate_w).sum(-1, keepdims=True))
    a = _A_ref(x, A_k_cache, A_v_cache, A_wq, A_wo)
    b = _B_ref(x, B_k_cache, B_v_cache, B_wq, B_wo)
    return g * a + (1 - g) * b
```

```cpp
#include <hip/hip_runtime.h>
#include <hip/hip_bf16.h>
#include <stdint.h>

typedef unsigned int u32;
typedef u32 u32x4 __attribute__((ext_vector_type(4)));
typedef float f32x4 __attribute__((ext_vector_type(4)));
typedef __bf16 bf16x2 __attribute__((ext_vector_type(2)));
typedef unsigned short u16;

constexpr int NB = 8, DM = 1024, NH = 32, SK = 4096, DH = 128, HKV = 4;

__device__ __forceinline__ float bflo(u32 x) { return __builtin_bit_cast(float, x << 16); }
__device__ __forceinline__ float bfhi(u32 x) { return __builtin_bit_cast(float, x & 0xffff0000u); }
__device__ __forceinline__ float bf2f(u16 x) { return __builtin_bit_cast(float, (u32)x << 16); }
__device__ __forceinline__ u16 f2bf(float f) { __bf16 b = (__bf16)f; return __builtin_bit_cast(u16, b); }
__device__ __forceinline__ float dot2(u32 a, u32 b, float c) {
  return __builtin_amdgcn_fdot2_f32_bf16(__builtin_bit_cast(bf16x2, a), __builtin_bit_cast(bf16x2, b), c, false);
}

__global__ void __launch_bounds__(256) k_qproj(const u16* __restrict__ x, const u16* __restrict__ wqa, const u16* __restrict__ wqb, u16* __restrict__ q) {
  __shared__ float xs[NB * DM];
  __shared__ float red[32][NB][33];
  const int tid = threadIdx.x;
  for (int i = tid; i < NB * DM; i += 256) xs[i] = bf2f(x[i]);
  __syncthreads();
  const int blk = blockIdx.x;
  const int br = blk >> 7, col0 = (blk & 127) * 32;
  const u16* w = br ? wqb : wqa;
  const int cg = tid & 3, rg = tid >> 2;
  float acc[NB][8];
#pragma unroll
  for (int b = 0; b < NB; b++)
#pragma unroll
    for (int j = 0; j < 8; j++) acc[b][j] = 0.f;
  u32x4 wv[16];
#pragma unroll
  for (int i = 0; i < 16; i++) wv[i] = *(const u32x4*)(w + (size_t)(rg + 64 * i) * 4096 + col0 + cg * 8);
#pragma unroll
  for (int i = 0; i < 16; i++) {
    const int r = rg + 64 * i;
    float wf[8];
#pragma unroll
    for (int k = 0; k < 4; k++) { wf[2 * k] = bflo(wv[i][k]); wf[2 * k + 1] = bfhi(wv[i][k]); }
#pragma unroll
    for (int b = 0; b < NB; b++) { const float xv = xs[b * DM + r];
#pragma unroll
      for (int j = 0; j < 8; j++) acc[b][j] = fmaf(xv, wf[j], acc[b][j]); }
  }
#pragma unroll
  for (int b = 0; b < NB; b++)
#pragma unroll
    for (int j = 0; j < 8; j++) {
      float v = acc[b][j];
      v += __shfl_xor(v, 4); v += __shfl_xor(v, 8); v += __shfl_xor(v, 16); v += __shfl_xor(v, 32);
      acc[b][j] = v;
    }
  const int wave = tid >> 6, lane = tid & 63;
  if (lane < 4) {
#pragma unroll
    for (int b = 0; b < NB; b++)
#pragma unroll
      for (int j = 0; j < 8; j++) red[lane * 8 + j][b][wave] = acc[b][j];
  }
  __syncthreads();
  { const int c = tid & 31, b = tid >> 5;
    const float v = red[c][b][0] + red[c][b][1] + red[c][b][2] + red[c][b][3];
    q[(size_t)(br * NB + b) * 4096 + col0 + c] = f2bf(v); }
}

template <int G>
__device__ __forceinline__ void attn_body(const u16* __restrict__ K, const u16* __restrict__ V, const u16* __restrict__ q, int nk,
                                          float* __restrict__ pm, float* __restrict__ pl, float* __restrict__ po, int pstride, float* lds) {
  const int tid = threadIdx.x, lane = tid & 63, wave = tid >> 6, r = lane >> 4, c = lane & 15;
  constexpr float C2 = 0.088388347648318440f * 1.4426950408889634f;
  u32x4 qp[G];
#pragma unroll
  for (int g = 0; g < G; g++) qp[g] = *(const u32x4*)(q + g * DH + 8 * c);
  float m[G], l[G], acc[G][8];
#pragma unroll
  for (int g = 0; g < G; g++) { m[g] = -1e30f; l[g] = 0.f;
#pragma unroll
    for (int k = 0; k < 8; k++) acc[g][k] = 0.f; }
  const int nw = blockDim.x >> 6;
  for (int t0 = wave * 16; t0 < nk; t0 += nw * 16) {
    u32x4 kc[4], vc[4];
#pragma unroll
    for (int t = 0; t < 4; t++) {
      kc[t] = __builtin_nontemporal_load((const u32x4*)(K + (size_t)(t0 + r + 4 * t) * DH + 8 * c));
      vc[t] = __builtin_nontemporal_load((const u32x4*)(V + (size_t)(t0 + r + 4 * t) * DH + 8 * c));
    }
#pragma unroll
    for (int g = 0; g < G; g++) {
      float s[4];
#pragma unroll
      for (int t = 0; t < 4; t++) {
        float d = 0.f;
#pragma unroll
        for (int k = 0; k < 4; k++) d = dot2(kc[t][k], qp[g][k], d);
        d += __shfl_xor(d, 1); d += __shfl_xor(d, 2); d += __shfl_xor(d, 4); d += __shfl_xor(d, 8);
        s[t] = d * C2;
      }
      const float mn = fmaxf(fmaxf(fmaxf(s[0], s[1]), fmaxf(s[2], s[3])), m[g]);
      const float al = __builtin_amdgcn_exp2f(m[g] - mn);
      m[g] = mn; l[g] *= al;
#pragma unroll
      for (int k = 0; k < 8; k++) acc[g][k] *= al;
#pragma unroll
      for (int t = 0; t < 4; t++) {
        const float p = __builtin_amdgcn_exp2f(s[t] - mn);
        l[g] += p;
#pragma unroll
        for (int k = 0; k < 4; k++) { acc[g][2 * k] = fmaf(p, bflo(vc[t][k]), acc[g][2 * k]); acc[g][2 * k + 1] = fmaf(p, bfhi(vc[t][k]), acc[g][2 * k + 1]); }
      }
    }
  }
  const int nrec = nw * 4;
  for (int g = 0; g < G; g++) {
    float* rec = lds + (wave * 4 + r) * 132;
    __syncthreads();
#pragma unroll
    for (int gg = 0; gg < G; gg++) if (gg == g) {
#pragma unroll
      for (int k = 0; k < 8; k++) rec[4 + 8 * c + k] = acc[gg][k];
      if (c == 0) { rec[0] = m[gg]; rec[1] = l[gg]; }
    }
    __syncthreads();
    if (tid < 128) {
      float M = -1e30f;
      for (int i = 0; i < nrec; i++) M = fmaxf(M, lds[i * 132]);
      float L = 0.f, o = 0.f;
      for (int i = 0; i < nrec; i++) { const float w = __builtin_amdgcn_exp2f(lds[i * 132] - M); L += w * lds[i * 132 + 1]; o += w * lds[i * 132 + 4 + tid]; }
      po[(size_t)g * pstride * DH + tid] = o;
      if (tid == 0) { pm[g * pstride] = M; pl[g * pstride] = L; }
    }
  }
}

constexpr int NSA = 2, NSB = 16;
__global__ void __launch_bounds__(512) k_attn(const u16* __restrict__ Ka, const u16* __restrict__ Va, const u16* __restrict__ Kb, const u16* __restrict__ Vb,
                                              const u16* __restrict__ q, float* __restrict__ pm, float* __restrict__ pl, float* __restrict__ po) {
  __shared__ float lds[32 * 132];
  int blk = blockIdx.x;
  if (blk < NB * HKV * NSB) {
    const int s = blk % NSB, kv = (blk / NSB) % HKV, b = blk / (NSB * HKV);
    const int nk = SK / NSB;
    const size_t off = ((size_t)(b * HKV + kv) * SK + s * nk) * DH;
    const int base = NB * NH * NSA + (b * NH + kv * 8) * NSB + s;
    attn_body<8>(Kb + off, Vb + off, q + (size_t)(NB + b) * 4096 + kv * 8 * DH, nk, pm + base, pl + base, po + (size_t)base * DH, NSB, lds);
  } else {
    blk -= NB * HKV * NSB;
    const int s = blk % NSA, h = (blk / NSA) % NH, b = blk / (NSA * NH);
    const int nk = SK / NSA;
    const size_t off = ((size_t)(b * NH + h) * SK + s * nk) * DH;
    const int base = (b * NH + h) * NSA + s;
    attn_body<1>(Ka + off, Va + off, q + (size_t)b * 4096 + h * DH, nk, pm + base, pl + base, po + (size_t)base * DH, NSA, lds);
  }
}

__global__ void __launch_bounds__(128) k_comb(const u16* __restrict__ x, const u16* __restrict__ gw, const float* __restrict__ pm, const float* __restrict__ pl, const float* __restrict__ po, float* __restrict__ o) {
  const int blk = blockIdx.x, tid = threadIdx.x;
  const int br = blk >> 8, b = (blk >> 5) & 7, h = blk & 31;
  const int ns = br ? NSB : NSA;
  const int base = br ? NB * NH * NSA + (b * NH + h) * NSB : (b * NH + h) * NSA;
  __shared__ float red[2];
  float z = 0.f;
  for (int i = tid; i < DM; i += 128) z += bf2f(x[b * DM + i]) * bf2f(gw[i]);
  for (int d = 32; d >= 1; d >>= 1) z += __shfl_xor(z, d);
  if ((tid & 63) == 0) red[tid >> 6] = z;
  __syncthreads();
  z = red[0] + red[1];
  const float g = 1.f / (1.f + __expf(-z));
  float M = -1e30f;
  for (int i = 0; i < ns; i++) M = fmaxf(M, pm[base + i]);
  float L = 0.f, acc = 0.f;
  for (int i = 0; i < ns; i++) { const float w = __builtin_amdgcn_exp2f(pm[base + i] - M); L += w * pl[base + i]; acc += w * po[(size_t)(base + i) * DH + tid]; }
  o[(size_t)(br * NB + b) * 4096 + h * DH + tid] = acc / L * (br ? 1.f - g : g);
}

__global__ void __launch_bounds__(512) k_out(const float* __restrict__ o, const u16* __restrict__ woa, const u16* __restrict__ wob, u16* __restrict__ out) {
  __shared__ float red[16][NB][8];
  const int tid = threadIdx.x, col0 = blockIdx.x * 8;
  float acc[NB][8];
#pragma unroll
  for (int b = 0; b < NB; b++)
#pragma unroll
    for (int j = 0; j < 8; j++) acc[b][j] = 0.f;
  u32x4 wv[16];
#pragma unroll
  for (int i = 0; i < 16; i++) { const int k = tid + 512 * i; const u16* w = (k < 4096) ? woa + (size_t)k * DM : wob + (size_t)(k - 4096) * DM; wv[i] = *(const u32x4*)(w + col0); }
#pragma unroll
  for (int i = 0; i < 16; i++) {
    const int k = tid + 512 * i; const int br = k >> 12, kk = k & 4095;
    float wf[8];
#pragma unroll
    for (int j = 0; j < 4; j++) { wf[2 * j] = bflo(wv[i][j]); wf[2 * j + 1] = bfhi(wv[i][j]); }
#pragma unroll
    for (int b = 0; b < NB; b++) { const float ov = o[(size_t)(br * NB + b) * 4096 + kk];
#pragma unroll
      for (int j = 0; j < 8; j++) acc[b][j] = fmaf(ov, wf[j], acc[b][j]); }
  }
#pragma unroll
  for (int b = 0; b < NB; b++)
#pragma unroll
    for (int j = 0; j < 8; j++) { float v = acc[b][j]; for (int d = 32; d >= 1; d >>= 1) v += __shfl_xor(v, d); acc[b][j] = v; }
  if ((tid & 63) == 0) {
#pragma unroll
    for (int b = 0; b < NB; b++)
#pragma unroll
      for (int j = 0; j < 8; j++) red[tid >> 6][b][j] = acc[b][j];
  }
  __syncthreads();
  if (tid < 64) { const int b = tid >> 3, j = tid & 7; float v = 0.f; for (int w = 0; w < 8; w++) v += red[w][b][j]; out[b * DM + col0 + j] = f2bf(v); }
}

extern "C" void kernel_launch(void* const* d_in, const int* in_sizes, int n_in, void* d_out, int out_size, void* d_ws, size_t ws_size, hipStream_t stream) {
  const u16* x = (const u16*)d_in[0];
  const u16* Ka = (const u16*)d_in[1]; const u16* Va = (const u16*)d_in[2];
  const u16* wqa = (const u16*)d_in[3]; const u16* woa = (const u16*)d_in[4];
  const u16* Kb = (const u16*)d_in[6]; const u16* Vb = (const u16*)d_in[7];
  const u16* wqb = (const u16*)d_in[8]; const u16* wob = (const u16*)d_in[9];
  const u16* gw = (const u16*)d_in[10];
  char* ws = (char*)d_ws;
  u16* q = (u16*)ws;
  const int nrec = NB * NH * (NSA + NSB);
  float* pm = (float*)(ws + (1 << 20)); float* pl = pm + nrec; float* po = (float*)(ws + (2 << 20));
  float* o = (float*)(ws + (8 << 20));
  k_qproj<<<256, 256, 0, stream>>>(x, wqa, wqb, q);
  k_attn<<<NB * HKV * NSB + NB * NH * NSA, 512, 0, stream>>>(Ka, Va, Kb, Vb, q, pm, pl, po);
  k_comb<<<512, 128, 0, stream>>>(x, gw, pm, pl, po, o);
  k_out<<<128, 512, 0, stream>>>(o, woa, wob, (u16*)d_out);
}
```

```cpp
#include <hip/hip_runtime.h>
#include <stdint.h>

typedef unsigned int u32;
typedef unsigned short u16;
typedef u32 u32x2 __attribute__((ext_vector_type(2)));
typedef u32 u32x4 __attribute__((ext_vector_type(4)));
typedef float f32x2 __attribute__((ext_vector_type(2)));
typedef float f32x4 __attribute__((ext_vector_type(4)));
typedef short s16x4 __attribute__((ext_vector_type(4)));
typedef __bf16 bf16x2 __attribute__((ext_vector_type(2)));
typedef __bf16 bf16x8 __attribute__((ext_vector_type(8)));
typedef __amdgpu_buffer_rsrc_t rsrc_t;
#define LAS __attribute__((address_space(3)))
#define DEVI __device__ __forceinline__

#define INF 22

constexpr int TILE = 16384, NSLOT = 8;
constexpr int L_SA = NSLOT * TILE;
constexpr int SBS = 1056;
constexpr int L_SB = L_SA + 8192;
constexpr int RS = 288;
constexpr int L_QA = L_SB + 8 * SBS;
constexpr int L_X = L_QA + 256;
constexpr int L_QB = L_X + 8 * RS;
constexpr int L_OB = L_QB + 8 * RS;
constexpr int L_OA = L_OB + 8 * RS;
constexpr int L_G = L_OA + 8 * RS;
constexpr int L_RED = L_G + 64;
constexpr int L_CTL = L_RED + 256;
constexpr int LDS_BYTES = L_CTL + 64;
constexpr int C_DONE = 0, C_FULL = 16, C_SYNC = 20, C_PUB = 24, C_RDY = 28;

constexpr u32 W_E = 0;
constexpr u32 W_QP = 4096;
constexpr u32 W_BP = W_QP + 2 * 256 * 8192;
constexpr u32 W_FP = W_BP + 256 * 8192;
constexpr u32 W_QF = W_FP + 256 * 8192;
constexpr u32 W_OB = W_QF + 256 * 512;
constexpr u32 W_OA = W_OB + 256 * 512;
constexpr u32 W_ML = W_OA + 256 * 512;
constexpr u32 W_G = W_ML + 256 * 128;
constexpr u32 W_END = W_G + 32 * 128;

constexpr int T_AK = 4, T_BK = 68, T_BV = 76, T_AV = 84, T_WO = 148, T_END = 152;

struct Args {
  const char* ph[8];
  const u16* x;
  const u16* gw;
  u16* out;
  char* ws;
};

DEVI rsrc_t mk(const void* p, u32 n) { return __builtin_amdgcn_make_buffer_rsrc((void*)p, 0, n, 0x00020000); }
DEVI u32 pack2(float a, float b) { f32x2 v = {a, b}; return __builtin_bit_cast(u32, __builtin_convertvector(v, bf16x2)); }
DEVI float blo(u32 u) { return __builtin_bit_cast(float, u << 16); }
DEVI float bhi(u32 u) { return __builtin_bit_cast(float, u & 0xffff0000u); }
DEVI float rbf(float a) { return blo(pack2(a, a)); }
DEVI float u2f(u32 u) { return __builtin_bit_cast(float, u); }
DEVI u32 f2u(float f) { return __builtin_bit_cast(u32, f); }
DEVI f32x4 mfma(u32x4 a, u32x4 b, f32x4 c) {
  return __builtin_amdgcn_mfma_f32_16x16x32_bf16(__builtin_bit_cast(bf16x8, a), __builtin_bit_cast(bf16x8, b), c, 0, 0, 0);
}
template <int CTRL> DEVI float dpp(float x) { return u2f(__builtin_amdgcn_mov_dpp(f2u(x), CTRL, 0xf, 0xf, true)); }
constexpr int XOR1 = 0xB1, XOR2 = 0x4E, XOR7 = 0x141, XOR15 = 0x140;
DEVI float sum16(float x) { x += dpp<XOR1>(x); x += dpp<XOR2>(x); x += dpp<XOR7>(x); x += dpp<XOR15>(x); return x; }
DEVI float x16_sum(float x) { auto s = __builtin_amdgcn_permlane16_swap(f2u(x), f2u(x), false, false); return u2f(s[0]) + u2f(s[1]); }
DEVI float x32_sum(float x) { auto s = __builtin_amdgcn_permlane32_swap(f2u(x), f2u(x), false, false); return u2f(s[0]) + u2f(s[1]); }
DEVI float x16_max(float x) { auto s = __builtin_amdgcn_permlane16_swap(f2u(x), f2u(x), false, false); return fmaxf(u2f(s[0]), u2f(s[1])); }
DEVI float x32_max(float x) { auto s = __builtin_amdgcn_permlane32_swap(f2u(x), f2u(x), false, false); return fmaxf(u2f(s[0]), u2f(s[1])); }
template <int N> DEVI void waitvm() { asm volatile("s_waitcnt vmcnt(%0)" ::"i"(N) : "memory"); }
DEVI void waitlds() { asm volatile("s_waitcnt lgkmcnt(0)" ::: "memory"); }

template <typename T> DEVI T ldl(u32 a) { return *(const LAS T*)(uintptr_t)a; }
template <typename T> DEVI void stl(u32 a, T v) { *(LAS T*)(uintptr_t)a = v; }
DEVI int ldctl(u32 a) { return __builtin_amdgcn_readfirstlane(*(volatile LAS int*)(uintptr_t)a); }
DEVI void stctl(u32 a, int v) { *(volatile LAS int*)(uintptr_t)a = v; }
DEVI void addctl(u32 a) { asm volatile("ds_add_u32 %0, %1" ::"v"(a), "v"(1) : "memory"); }

constexpr int LCAP = 1 << 16, GCAP = 1 << 12;

DEVI void wait_ctl(u32 a, int want, int& seen, int& budget) {
  while (seen < want) {
    seen = ldctl(a);
    if (seen >= want) break;
    if (--budget < 0) break;
    __builtin_amdgcn_s_sleep(1);
  }
  asm volatile("" ::: "memory");
}

#define STAMP(i) do { } while (0)

__global__ void __launch_bounds__(384, 2) fused_decode(Args a) {
  __shared__ __attribute__((aligned(1024))) char lds[LDS_BYTES];
  const u32 L = (u32)(uintptr_t)(LAS char*)lds;
  const int wave = __builtin_amdgcn_readfirstlane(threadIdx.x >> 6), lane = threadIdx.x & 63;
  const int wg = blockIdx.x, h = (wg & 7) * 4 + ((wg >> 3) & 3), b = wg >> 5, cu = b * 32 + h;
  const int g = lane >> 4, c = lane & 15;

  if (wave == 0) {
    __builtin_amdgcn_s_setprio(3);
    STAMP(0);
    if (lane < 16) stctl(L + L_CTL + 4 * lane, 0);
    int t = 0, freed = 0, pubd = 0, budget = LCAP;
    const char* nxt = a.ph[0];
    for (int ph = 0; ph < 8; ph++) {
      const char* base = nxt;
      nxt = a.ph[(ph + 1) & 7];
      const bool isq = ph < 2, iso = ph >= 6, isk = ph == 2 || ph == 3, big = ph == 2 || ph == 5;
      const u32 S = isq ? 8192 : iso ? 2048 : 256;
      const u32 off = isq ? (u32)(b * 128 * 8192 + h * 256) : iso ? (u32)(h * 128 * 2048 + b * 256) : big ? (u32)cu << 20 : (u32)cu << 17;
      const int nt = (isq || iso) ? 2 : big ? 64 : 8;
      const rsrc_t r = mk(base + off, (u32)(nt * 64 - 1) * S + 256);
      int vo[4];
#pragma unroll
      for (int j = 0; j < 4; j++) vo[j] = g * S + ((c ^ (isk ? 4 * j + g : (g | ((j >> 1) << 2)) << 1)) << 4);
      u32 so = 0;
      for (int i = 0; i < nt; i++, t++) {
        while (freed < t - (NSLOT - 1)) {
          u32x4 d;
          asm volatile("ds_read_b128 %0, %1\n\ts_waitcnt lgkmcnt(0)" : "=v"(d) : "v"(L + L_CTL + C_DONE) : "memory");
          freed = __builtin_amdgcn_readfirstlane(min(min((int)d[0], (int)d[1]), min((int)d[2], (int)d[3])));
          if (freed >= t - (NSLOT - 1)) break;
          if (--budget < 0) break;
          waitvm<0>();
          pubd = t;
          asm volatile("ds_write_b32 %0, %1" ::"v"(L + L_CTL + C_FULL), "v"(t) : "memory");
          __builtin_amdgcn_s_sleep(1);
        }
        LAS char* dst = (LAS char*)lds + (t & (NSLOT - 1)) * TILE;
#pragma unroll
        for (int u = 0; u < 16; u++) {
          waitvm<INF - 1>();
          if (u == ((INF - 1) & 15)) asm volatile("ds_write_b32 %0, %1" ::"v"(L + L_CTL + C_FULL), "v"(max(pubd, t + 1 - ((INF + 15) >> 4))) : "memory");
          __builtin_amdgcn_raw_ptr_buffer_load_lds(r, (LAS void*)(dst + u * 1024), 16, vo[u & 3], so, 0, 2);
          so += 4 * S;
        }
        if (t == 0) { waitlds(); __builtin_amdgcn_s_barrier(); }
      }
    }
    STAMP(1);
    if (INF > 32) { waitvm<32>(); asm volatile("ds_write_b32 %0, %1" ::"v"(L + L_CTL + C_FULL), "v"(max(pubd, T_END - 2)) : "memory"); }
    waitvm<16>();
    asm volatile("ds_write_b32 %0, %1" ::"v"(L + L_CTL + C_FULL), "v"(T_END - 1) : "memory");
    waitvm<0>();
    asm volatile("ds_write_b32 %0, %1" ::"v"(L + L_CTL + C_FULL), "v"(T_END) : "memory");
    STAMP(2);
    return;
  }

  const rsrc_t rw = mk(a.ws, W_END);
  __builtin_amdgcn_s_barrier();
  const u32 tagraw = __builtin_amdgcn_raw_buffer_load_b32(rw, 0, W_E, 16);

  if (wave < 5) {
    const int w = wave - 1;
    STAMP(0);
    {
      const u32x4 x0 = *(const u32x4*)(a.x + g * 1024 + b * 128 + c * 8);
      const u32x4 x1 = *(const u32x4*)(a.x + (g + 4) * 1024 + b * 128 + c * 8);
      stl<u32x4>(L + L_X + g * RS + c * 16, x0);
      stl<u32x4>(L + L_X + (g + 4) * RS + c * 16, x1);
    }
    const u32 tag = __builtin_amdgcn_readfirstlane(tagraw) + 1u;
    int full = 0, rdy = 0, syn = 0, nsync = 0, budget = LCAP;
    const u32 f = ((lane & 15) >> 2) | ((g & 1) << 2);
    const u32 tr0 = L + (8 * g + ((lane & 15) >> 2)) * 256 + 32 * ((2 * w) ^ f) + 8 * (lane & 3);
    const u32 tr1 = tr0 ^ 32;
    const u32 kr0 = L + (16 * w + c) * 256 + ((g ^ c) << 4);
    f32x4 acc0, acc1;

    auto csync = [&]() {
      waitlds();
      if (lane == 0) addctl(L + L_CTL + C_SYNC);
      nsync += 4;
      wait_ctl(L + L_CTL + C_SYNC, nsync, syn, budget);
    };
    auto gemv = [&](int t0, int t1, u32 ba) {
      acc0 = f32x4{0.f, 0.f, 0.f, 0.f}; acc1 = acc0;
      for (int t = t0; t < t1; t++, ba += 128) {
        wait_ctl(L + L_CTL + C_FULL, t + 1, full, budget);
        const u32 sb = (t & (NSLOT - 1)) * TILE;
        s16x4 v[8];
#pragma unroll
        for (int i = 0; i < 8; i++)
          v[i] = __builtin_amdgcn_ds_read_tr16_b64_v4i16((LAS s16x4*)(uintptr_t)(((i & 4) ? tr1 : tr0) + sb + ((i >> 1) & 1) * 8192 + (i & 1) * 1024));
        const u32x4 p0 = ldl<u32x4>(ba), p1 = ldl<u32x4>(ba + 64);
        waitlds();
        stctl(L + L_CTL + C_DONE + 4 * w, t + 1);
        u32x4 fr[4];
#pragma unroll
        for (int i = 0; i < 4; i++) {
          const u32x2 lo = __builtin_bit_cast(u32x2, v[2 * i]), hi = __builtin_bit_cast(u32x2, v[2 * i + 1]);
          fr[i] = u32x4{lo[0], lo[1], hi[0], hi[1]};
        }
        acc0 = mfma(fr[0], p0, acc0); acc1 = mfma(fr[2], p0, acc1);
        acc0 = mfma(fr[1], p1, acc0); acc1 = mfma(fr[3], p1, acc1);
      }
    };
    auto publish = [&](u32 off) {
      const f32x4 v = (lane & 8) ? acc1 : acc0;
      __builtin_amdgcn_raw_buffer_store_b128(u32x4{f2u(v[0]), tag, f2u(v[1]), tag}, rw, off, 0, 16);
      __builtin_amdgcn_raw_buffer_store_b128(u32x4{f2u(v[2]), tag, f2u(v[3]), tag}, rw, off + 16, 0, 16);
    };
    float mx;
    auto qk = [&](int t0, int t1, u32 qa, u32 sa, bool wr) {
      u32x4 q[4];
#pragma unroll
      for (int s = 0; s < 4; s++) q[s] = ldl<u32x4>(qa + 64 * s);
      mx = -3.0e38f;
      for (int t = t0; t < t1; t++, sa += 128) {
        wait_ctl(L + L_CTL + C_FULL, t + 1, full, budget);
        const u32 ka = kr0 + (t & (NSLOT - 1)) * TILE;
        u32x4 k[4];
#pragma unroll
        for (int s = 0; s < 4; s++) k[s] = ldl<u32x4>(ka ^ (s << 6));
        waitlds();
        stctl(L + L_CTL + C_DONE + 4 * w, t + 1);
        f32x4 d = {0.f, 0.f, 0.f, 0.f};
#pragma unroll
        for (int s = 0; s < 4; s++) d = mfma(k[s], q[s], d);
        constexpr float SC = 0.08838834764831845f;
        const u32 u01 = pack2(d[0], d[1]), u23 = pack2(d[2], d[3]);
        const float s0 = blo(u01) * SC, s1 = bhi(u01) * SC, s2 = blo(u23) * SC, s3 = bhi(u23) * SC;
        mx = fmaxf(fmaxf(mx, fmaxf(s0, s1)), fmaxf(s2, s3));
        if (wr) stl<u32x2>(sa, u32x2{pack2(s0, s1), pack2(s2, s3)});
      }
      mx = rbf(x32_max(x16_max(mx)));
    };
    constexpr float LOG2E = 1.4426950408889634f;
    const u32 colofs = (32 * w + 2 * (lane & 8) + 4 * g) * 8;
    const u32 xop = (lane & 7) * RS + 16 * g;

    gemv(0, 2, L + L_X + xop);
    publish(W_QP + ((lane & 7) * 32 + h) * 8192 + b * 1024 + colofs);
    if (lane == 0) addctl(L + L_CTL + C_PUB);
    STAMP(1);
    gemv(2, 4, L + L_X + xop);
    publish(W_QP + 256 * 8192 + ((lane & 7) * 32 + h) * 8192 + b * 1024 + colofs);
    wait_ctl(L + L_CTL + C_RDY, 1, rdy, budget);
    STAMP(2);
    qk(T_AK, T_BK, L + L_QA + 16 * g, L + L_SA + (16 * w + 4 * g) * 2, c == 0);
    STAMP(3);
    if (lane == 0) stl<float>(L + L_RED + 4 * w, mx);
    csync();
    {
      const f32x4 m4 = ldl<f32x4>(L + L_RED);
      const float M = fmaxf(fmaxf(m4[0], m4[1]), fmaxf(m4[2], m4[3]));
      const u32 pa = L + L_SA + w * 2048 + lane * 16;
      const u32x4 s0 = ldl<u32x4>(pa), s1 = ldl<u32x4>(pa + 1024);
      float e[16], sum = 0.f;
#pragma unroll
      for (int i = 0; i < 4; i++) {
        e[2 * i] = __builtin_amdgcn_exp2f((blo(s0[i]) - M) * LOG2E); e[2 * i + 1] = __builtin_amdgcn_exp2f((bhi(s0[i]) - M) * LOG2E);
        e[8 + 2 * i] = __builtin_amdgcn_exp2f((blo(s1[i]) - M) * LOG2E); e[9 + 2 * i] = __builtin_amdgcn_exp2f((bhi(s1[i]) - M) * LOG2E);
      }
#pragma unroll
      for (int i = 0; i < 16; i++) sum += e[i];
      sum = x32_sum(x16_sum(sum16(sum)));
      if (lane == 0) stl<float>(L + L_RED + 16 + 4 * w, sum);
      csync();
      const f32x4 l4 = ldl<f32x4>(L + L_RED + 16);
      const float inv = 1.0f / ((l4[0] + l4[1]) + (l4[2] + l4[3]));
      u32x4 p0, p1;
#pragma unroll
      for (int i = 0; i < 4; i++) { p0[i] = pack2(e[2 * i] * inv, e[2 * i + 1] * inv); p1[i] = pack2(e[8 + 2 * i] * inv, e[9 + 2 * i] * inv); }
      stl<u32x4>(pa, p0); stl<u32x4>(pa + 1024, p1);
    }
    STAMP(4);
    wait_ctl(L + L_CTL + C_RDY, 2, rdy, budget);
    qk(T_BK, T_BV, L + L_QB + xop, L + L_SB + (lane & 7) * SBS + (16 * w + 4 * g) * 2, c < 8);
    if (lane < 8) stl<float>(L + L_RED + 64 + lane * 16 + 4 * w, mx);
    csync();
    {
      const int hh = 2 * w + (lane >> 5);
      const f32x4 m4 = ldl<f32x4>(L + L_RED + 64 + hh * 16);
      const float M = fmaxf(fmaxf(m4[0], m4[1]), fmaxf(m4[2], m4[3]));
      const u32 pa = L + L_SB + hh * SBS + (lane & 31) * 16;
      const u32x4 s0 = ldl<u32x4>(pa), s1 = ldl<u32x4>(pa + 512);
      float sum = 0.f;
      u32x4 p0, p1;
#pragma unroll
      for (int i = 0; i < 4; i++) {
        const float e0 = __builtin_amdgcn_exp2f((blo(s0[i]) - M) * LOG2E), e1 = __builtin_amdgcn_exp2f((bhi(s0[i]) - M) * LOG2E);
        const float e2 = __builtin_amdgcn_exp2f((blo(s1[i]) - M) * LOG2E), e3 = __builtin_amdgcn_exp2f((bhi(s1[i]) - M) * LOG2E);
        sum += (e0 + e1) + (e2 + e3);
        p0[i] = pack2(e0, e1); p1[i] = pack2(e2, e3);
      }
      stl<u32x4>(pa, p0); stl<u32x4>(pa + 512, p1);
      sum = x16_sum(sum16(sum));
      if ((lane & 31) == 0)
        __builtin_amdgcn_raw_buffer_store_b128(u32x4{f2u(M), tag, f2u(sum), tag}, rw, W_ML + ((cu & ~7) + hh) * 128 + (h & 7) * 16, 0, 16);
    }
    csync();
    gemv(T_BV, T_AV, L + L_SB + (lane & 7) * SBS + 16 * g);
    publish(W_BP + ((cu & ~7) + (lane & 7)) * 8192 + (h & 7) * 1024 + colofs);
    STAMP(5);
    gemv(T_AV, T_WO, L + L_SA + 16 * g);
    STAMP(6);
    if (c < 2) {
      const f32x4 v = c ? acc1 : acc0;
      __builtin_amdgcn_raw_buffer_store_b128(u32x4{pack2(v[0], v[1]), tag, pack2(v[2], v[3]), tag}, rw,
                                             W_OA + (h * 8 + b) * 512 + (32 * w + 16 * c + 4 * g) * 4, 0, 16);
    }
    if (lane == 0) addctl(L + L_CTL + C_PUB);
    wait_ctl(L + L_CTL + C_RDY, 3, rdy, budget);
    gemv(T_WO, T_WO + 2, L + L_OB + xop);
    const f32x2 gg = ldl<f32x2>(L + L_G + (lane & 7) * 8);
    const f32x4 b0 = acc0 * gg[1], b1 = acc1 * gg[1];
    wait_ctl(L + L_CTL + C_RDY, 4, rdy, budget);
    STAMP(7);
    gemv(T_WO + 2, T_END, L + L_OA + xop);
    acc0 = acc0 * gg[0] + b0; acc1 = acc1 * gg[0] + b1;
    publish(W_FP + ((lane & 7) * 32 + 4 * b + w) * 8192 + h * 256 + (2 * (lane & 8) + 4 * g) * 8);
    if (lane == 0) addctl(L + L_CTL + C_PUB);
    STAMP(8);
    return;
  }

  {
    STAMP(0);
    const u32x4 xa = *(const u32x4*)(a.x + b * 1024 + lane * 8), xb = *(const u32x4*)(a.x + b * 1024 + 512 + lane * 8);
    const u32x4 ga = *(const u32x4*)(a.gw + lane * 8), gb = *(const u32x4*)(a.gw + 512 + lane * 8);
    const u32 tag = __builtin_amdgcn_readfirstlane(tagraw) + 1u;
    int full = 0, pub = 0, budget = LCAP, gbudget = GCAP;
    u32x4 d[8];
    auto gather8 = [&](u32 off) {
      for (;;) {
        bool ok = true;
#pragma unroll
        for (int i = 0; i < 8; i++) d[i] = __builtin_amdgcn_raw_buffer_load_b128(rw, lane * 16 + i * 1024, off, 16);
#pragma unroll
        for (int i = 0; i < 8; i++) ok &= (d[i][1] == tag) & (d[i][3] == tag);
        if (__all(ok)) break;
        if (--gbudget < 0) break;
        __builtin_amdgcn_s_sleep(2);
        asm volatile("" ::: "memory");
      }
    };
    auto gather4 = [&](u32 off, u32 img, bool extra, u32 eoff) {
      for (;;) {
        bool ok = true;
#pragma unroll
        for (int i = 0; i < 4; i++) d[i] = __builtin_amdgcn_raw_buffer_load_b128(rw, lane * 16 + i * 1024, off, 16);
        if (extra) d[4] = __builtin_amdgcn_raw_buffer_load_b128(rw, (lane & 7) * 16, eoff, 16);
#pragma unroll
        for (int i = 0; i < 4; i++) ok &= (d[i][1] == tag) & (d[i][3] == tag);
        if (extra) ok &= (d[4][1] == tag) & (d[4][3] == tag);
        if (__all(ok)) break;
        if (--gbudget < 0) break;
        __builtin_amdgcn_s_sleep(2);
        asm volatile("" ::: "memory");
      }
#pragma unroll
      for (int i = 0; i < 4; i++) stl<u32x2>(img + (2 * i + (lane >> 5)) * RS + (lane & 31) * 8, u32x2{d[i][0], d[i][2]});
    };
    auto sum8 = [&](float& s0, float& s1) {
      s0 = ((u2f(d[0][0]) + u2f(d[1][0])) + (u2f(d[2][0]) + u2f(d[3][0]))) + ((u2f(d[4][0]) + u2f(d[5][0])) + (u2f(d[6][0]) + u2f(d[7][0])));
      s1 = ((u2f(d[0][2]) + u2f(d[1][2])) + (u2f(d[2][2]) + u2f(d[3][2]))) + ((u2f(d[4][2]) + u2f(d[5][2])) + (u2f(d[6][2]) + u2f(d[7][2])));
    };
    float s0, s1;
    wait_ctl(L + L_CTL + C_PUB, 4, pub, budget);
    gather8(W_QP + cu * 8192);
    sum8(s0, s1);
    stl<u32>(L + L_QA + lane * 4, pack2(s0, s1));
    waitlds();
    stctl(L + L_CTL + C_RDY, 1);
    STAMP(1);
    float z = 0.f;
#pragma unroll
    for (int i = 0; i < 4; i++) {
      const u32 pa = pack2(blo(xa[i]) * blo(ga[i]), bhi(xa[i]) * bhi(ga[i])), pb = pack2(blo(xb[i]) * blo(gb[i]), bhi(xb[i]) * bhi(gb[i]));
      z += (blo(pa) + bhi(pa)) + (blo(pb) + bhi(pb));
    }
    z = rbf(x32_sum(x16_sum(sum16(z))));
    const float gate = rbf(1.0f / (1.0f + __expf(-z))), gate1 = rbf(1.0f - gate);
    wait_ctl(L + L_CTL + C_FULL, 12, full, budget);
    gather8(W_QP + 256 * 8192 + cu * 8192);
    sum8(s0, s1);
    __builtin_amdgcn_raw_buffer_store_b64(u32x2{pack2(s0, s1), tag}, rw, lane * 8, W_QF + cu * 512, 16);
    wait_ctl(L + L_CTL + C_FULL, 32, full, budget);
    gather4(W_QF + (cu & ~7) * 512, L + L_QB, false, 0);
    waitlds();
    stctl(L + L_CTL + C_RDY, 2);
    STAMP(2);
    wait_ctl(L + L_CTL + C_FULL, T_AV + 12, full, budget);
    {
      u32x4 ml;
      for (;;) {
        ml = __builtin_amdgcn_raw_buffer_load_b128(rw, (lane & 7) * 16, W_ML + cu * 128, 16);
        if (__all((ml[1] == tag) & (ml[3] == tag))) break;
        if (--gbudget < 0) break;
        __builtin_amdgcn_s_sleep(2);
        asm volatile("" ::: "memory");
      }
      gather8(W_BP + cu * 8192);
      float M = u2f(ml[0]);
      M = fmaxf(M, dpp<XOR1>(M)); M = fmaxf(M, dpp<XOR2>(M)); M = fmaxf(M, dpp<XOR7>(M));
      const float wt = __builtin_amdgcn_exp2f((u2f(ml[0]) - M) * 1.4426950408889634f);
      float Ls = wt * u2f(ml[2]);
      Ls += dpp<XOR1>(Ls); Ls += dpp<XOR2>(Ls); Ls += dpp<XOR7>(Ls);
      const float wn = wt / Ls;
      s0 = 0.f; s1 = 0.f;
#pragma unroll
      for (int i = 0; i < 8; i++) {
        const float wi = u2f(__builtin_amdgcn_readlane(f2u(wn), i));
        s0 = fmaf(wi, u2f(d[i][0]), s0); s1 = fmaf(wi, u2f(d[i][2]), s1);
      }
      __builtin_amdgcn_raw_buffer_store_b64(u32x2{pack2(s0, s1), tag}, rw, lane * 8, W_OB + (h * 8 + b) * 512, 16);
      if (lane == 0) __builtin_amdgcn_raw_buffer_store_b128(u32x4{f2u(gate), tag, f2u(gate1), tag}, rw, 0, W_G + h * 128 + b * 16, 16);
    }
    wait_ctl(L + L_CTL + C_FULL, T_AV + 28, full, budget);
    gather4(W_OB + h * 4096, L + L_OB, true, W_G + h * 128);
    stl<u32x2>(L + L_G + (lane & 7) * 8, u32x2{d[4][0], d[4][2]});
    waitlds();
    stctl(L + L_CTL + C_RDY, 3);
    STAMP(3);
    wait_ctl(L + L_CTL + C_PUB, 8, pub, budget);
    STAMP(4);
    gather4(W_OA + h * 4096, L + L_OA, false, 0);
    waitlds();
    stctl(L + L_CTL + C_RDY, 4);
    STAMP(5);
    wait_ctl(L + L_CTL + C_PUB, 12, pub, budget);
    STAMP(6);
    gather8(W_FP + cu * 8192);
    sum8(s0, s1);
    s0 = x32_sum(x16_sum(s0)); s1 = x32_sum(x16_sum(s1));
    if (lane < 16) *(u32*)(a.out + b * 1024 + h * 32 + lane * 2) = pack2(s0, s1);
    if (wg == 0 && lane == 0) __builtin_amdgcn_raw_buffer_store_b32(tag, rw, 0, W_E, 16);
    STAMP(7);
  }
}

extern "C" void kernel_launch(void* const* d_in, const int* in_sizes, int n_in, void* d_out, int out_size, void* d_ws, size_t ws_size,
                              hipStream_t stream) {
  Args a;
  a.ph[0] = (const char*)d_in[3];
  a.ph[1] = (const char*)d_in[8];
  a.ph[2] = (const char*)d_in[1];
  a.ph[3] = (const char*)d_in[6];
  a.ph[4] = (const char*)d_in[7];
  a.ph[5] = (const char*)d_in[2];
  a.ph[6] = (const char*)d_in[9];
  a.ph[7] = (const char*)d_in[4];
  a.x = (const u16*)d_in[0];
  a.gw = (const u16*)d_in[10];
  a.out = (u16*)d_out;
  a.ws = (char*)d_ws;
  fused_decode<<<256, 384, 0, stream>>>(a);
}
```

```cpp
#include <hip/hip_runtime.h>
#include <stdint.h>

typedef unsigned int u32;
typedef unsigned short u16;
typedef u32 u32x2 __attribute__((ext_vector_type(2)));
typedef u32 u32x4 __attribute__((ext_vector_type(4)));
typedef float f32x2 __attribute__((ext_vector_type(2)));
typedef float f32x4 __attribute__((ext_vector_type(4)));
typedef short s16x4 __attribute__((ext_vector_type(4)));
typedef __bf16 bf16x2 __attribute__((ext_vector_type(2)));
typedef __bf16 bf16x8 __attribute__((ext_vector_type(8)));
typedef __amdgpu_buffer_rsrc_t rsrc_t;
#define LAS __attribute__((address_space(3)))
#define DEVI __device__ __forceinline__

#define INF 20

#define X_NOPUB 0
#define X_NOGAT 0
#define X_STAUX 18
#define X_LDAUX 18
#define TBITS 8
#define QTRIG 7
#define X_SMALLAUX 16
#define X_FPST 16
#define X_FPLD 16
#define SLP 1
constexpr int TILE = 16384, NSLOT = 8;
constexpr int L_SA = NSLOT * TILE;
constexpr int SBS = 1056;
constexpr int L_SB = L_SA + 8192;
constexpr int RS = 288;
constexpr int L_QA = L_SB + 8 * SBS;
constexpr int L_X = L_QA + 256;
constexpr int L_QB = L_X + 8 * RS;
constexpr int L_OB = L_QB + 8 * RS;
constexpr int L_OA = L_OB + 8 * RS;
constexpr int L_G = L_OA + 8 * RS;
constexpr int L_RED = L_G + 64;
constexpr int L_CTL = L_RED + 256;
constexpr int L_ST = L_CTL + 64;
constexpr int LDS_BYTES = L_ST;
constexpr int C_DONE = 0, C_FULL = 16, C_SYNC = 20, C_PUB = 24, C_RDY = 28;

constexpr u32 W_E = 0;
constexpr u32 W_QP = 4096;
constexpr u32 W_BP = W_QP + 2 * 256 * 4096;
constexpr u32 W_FP = W_BP + 256 * 4096;
constexpr u32 W_QF = W_FP + 256 * 4096;
constexpr u32 W_OB = W_QF + 256 * 512;
constexpr u32 W_OA = W_OB + 256 * 512;
constexpr u32 W_ML = W_OA + 256 * 512;
constexpr u32 W_G = W_ML + 256 * 128;
constexpr u32 W_END = W_G + 32 * 128;
constexpr u32 TMASK = (1u << TBITS) - 1;

constexpr int T_AK = 4, T_BK = 68, T_BV = 76, T_AV = 84, T_WO = 148, T_END = 152;

struct Args {
  const char* ph[8];
  const u16* x;
  const u16* gw;
  u16* out;
  char* ws;
};

DEVI rsrc_t mk(const void* p, u32 n) { return __builtin_amdgcn_make_buffer_rsrc((void*)p, 0, n, 0x00020000); }
DEVI u32 pack2(float a, float b) { f32x2 v = {a, b}; return __builtin_bit_cast(u32, __builtin_convertvector(v, bf16x2)); }
DEVI float blo(u32 u) { return __builtin_bit_cast(float, u << 16); }
DEVI float bhi(u32 u) { return __builtin_bit_cast(float, u & 0xffff0000u); }
DEVI float rbf(float a) { return blo(pack2(a, a)); }
DEVI float u2f(u32 u) { return __builtin_bit_cast(float, u); }
DEVI u32 f2u(float f) { return __builtin_bit_cast(u32, f); }
DEVI f32x4 mfma(u32x4 a, u32x4 b, f32x4 c) {
  return __builtin_amdgcn_mfma_f32_16x16x32_bf16(__builtin_bit_cast(bf16x8, a), __builtin_bit_cast(bf16x8, b), c, 0, 0, 0);
}
template <int CTRL> DEVI float dpp(float x) { return u2f(__builtin_amdgcn_mov_dpp(f2u(x), CTRL, 0xf, 0xf, true)); }
constexpr int XOR1 = 0xB1, XOR2 = 0x4E, XOR7 = 0x141, XOR15 = 0x140;
DEVI float sum16(float x) { x += dpp<XOR1>(x); x += dpp<XOR2>(x); x += dpp<XOR7>(x); x += dpp<XOR15>(x); return x; }
DEVI float x16_sum(float x) { auto s = __builtin_amdgcn_permlane16_swap(f2u(x), f2u(x), false, false); return u2f(s[0]) + u2f(s[1]); }
DEVI float x32_sum(float x) { auto s = __builtin_amdgcn_permlane32_swap(f2u(x), f2u(x), false, false); return u2f(s[0]) + u2f(s[1]); }
DEVI float x16_max(float x) { auto s = __builtin_amdgcn_permlane16_swap(f2u(x), f2u(x), false, false); return fmaxf(u2f(s[0]), u2f(s[1])); }
DEVI float x32_max(float x) { auto s = __builtin_amdgcn_permlane32_swap(f2u(x), f2u(x), false, false); return fmaxf(u2f(s[0]), u2f(s[1])); }
template <int N> DEVI void waitvm() { asm volatile("s_waitcnt vmcnt(%0)" ::"i"(N) : "memory"); }
DEVI void waitlds() { asm volatile("s_waitcnt lgkmcnt(0)" ::: "memory"); }

template <typename T> DEVI T ldl(u32 a) { return *(const LAS T*)(uintptr_t)a; }
template <typename T> DEVI void stl(u32 a, T v) { *(LAS T*)(uintptr_t)a = v; }
DEVI int ldctl(u32 a) { return __builtin_amdgcn_readfirstlane(*(volatile LAS int*)(uintptr_t)a); }
DEVI void stctl(u32 a, int v) { *(volatile LAS int*)(uintptr_t)a = v; }
DEVI void addctl(u32 a) { asm volatile("ds_add_u32 %0, %1" ::"v"(a), "v"(1) : "memory"); }

constexpr int LCAP = 1 << 16, GCAP = 1 << 12;

DEVI void wait_ctl(u32 a, int want, int& seen, int& budget) {
  while (seen < want) {
    seen = ldctl(a);
    if (seen >= want) break;
    if (--budget < 0) break;
    __builtin_amdgcn_s_sleep(SLP);
  }
  asm volatile("" ::: "memory");
}

#define STAMPV(i, v) do { } while (0)
#define STAMP(i) do { } while (0)
#define STAMP_INIT() do { } while (0)
#define STAMP_DUMP() do { } while (0)

__global__ void __launch_bounds__(384, 2) fused_decode(Args a) {
  __shared__ __attribute__((aligned(1024))) char lds[LDS_BYTES];
  const u32 L = (u32)(uintptr_t)(LAS char*)lds;
  const int wave = __builtin_amdgcn_readfirstlane(threadIdx.x >> 6), lane = threadIdx.x & 63;
  const int wg = blockIdx.x, h = (wg & 7) * 4 + ((wg >> 3) & 3), b = wg >> 5, cu = b * 32 + h;
  const int g = lane >> 4, c = lane & 15;

  STAMP_INIT();
  if (wave == 0) {
    __builtin_amdgcn_s_setprio(3);
    STAMP(0);
    if (lane < 16) stctl(L + L_CTL + 4 * lane, 0);
    int t = 0, freed = 0, pubd = 0, budget = LCAP;
    const char* nxt = a.ph[0];
    for (int ph = 0; ph < 8; ph++) {
      const char* base = nxt;
      nxt = a.ph[(ph + 1) & 7];
      const bool isq = ph < 2, iso = ph >= 6, isk = ph == 2 || ph == 3, big = ph == 2 || ph == 5;
      const u32 S = isq ? 8192 : iso ? 2048 : 256;
      const u32 off = isq ? (u32)(b * 128 * 8192 + h * 256) : iso ? (u32)(h * 128 * 2048 + b * 256) : big ? (u32)cu << 20 : (u32)cu << 17;
      const int nt = (isq || iso) ? 2 : big ? 64 : 8;
      const rsrc_t r = mk(base + off, (u32)(nt * 64 - 1) * S + 256);
      int vo[4];
#pragma unroll
      for (int j = 0; j < 4; j++)
        vo[j] = g * S + ((c ^ (isk ? 4 * j + g : (g | ((j >> 1) << 2)) << 1)) << 4);
      u32 so = 0;
      for (int i = 0; i < nt; i++, t++) {
        while (freed < t - (NSLOT - 1)) {
          u32x4 d;
          asm volatile("ds_read_b128 %0, %1\n\ts_waitcnt lgkmcnt(0)" : "=v"(d) : "v"(L + L_CTL + C_DONE) : "memory");
          freed = __builtin_amdgcn_readfirstlane(min(min((int)d[0], (int)d[1]), min((int)d[2], (int)d[3])));
          if (freed >= t - (NSLOT - 1)) break;
          if (--budget < 0) break;
          waitvm<0>();
          pubd = t;
          asm volatile("ds_write_b32 %0, %1" ::"v"(L + L_CTL + C_FULL), "v"(t) : "memory");
          __builtin_amdgcn_s_sleep(1);
        }
        LAS char* dst = (LAS char*)lds + (t & (NSLOT - 1)) * TILE;
#pragma unroll
        for (int u = 0; u < 16; u++) {
          waitvm<INF - 1>();
          if (u == ((INF - 1) & 15)) asm volatile("ds_write_b32 %0, %1" ::"v"(L + L_CTL + C_FULL), "v"(max(pubd, t + 1 - ((INF + 15) >> 4))) : "memory");
          __builtin_amdgcn_raw_ptr_buffer_load_lds(r, (LAS void*)(dst + u * 1024), 16, vo[u & 3], so, 0, 2);
          so += 4 * S;
        }
        if (t == 0) { waitlds(); __builtin_amdgcn_s_barrier(); }
      }
    }
    STAMP(1);
    if (INF > 32) { waitvm<32>(); asm volatile("ds_write_b32 %0, %1" ::"v"(L + L_CTL + C_FULL), "v"(max(pubd, T_END - 2)) : "memory"); }
    waitvm<16>();
    asm volatile("ds_write_b32 %0, %1" ::"v"(L + L_CTL + C_FULL), "v"(T_END - 1) : "memory");
    waitvm<0>();
    asm volatile("ds_write_b32 %0, %1" ::"v"(L + L_CTL + C_FULL), "v"(T_END) : "memory");
    STAMP(2);
    STAMP_DUMP();
    return;
  }

  const rsrc_t rw = mk(a.ws, W_END);
  __builtin_amdgcn_s_barrier();
  const u32 tagraw = __builtin_amdgcn_raw_buffer_load_b32(rw, 0, W_E, 16);

  if (wave < 5) {
    const int w = wave - 1;
    STAMP(0);
    {
      const u32x4 x0 = *(const u32x4*)(a.x + g * 1024 + b * 128 + c * 8);
      const u32x4 x1 = *(const u32x4*)(a.x + (g + 4) * 1024 + b * 128 + c * 8);
      stl<u32x4>(L + L_X + g * RS + c * 16, x0);
      stl<u32x4>(L + L_X + (g + 4) * RS + c * 16, x1);
    }
    const u32 tag = __builtin_amdgcn_readfirstlane(tagraw) + 1u;
    int full = 0, rdy = 0, syn = 0, nsync = 0, budget = LCAP;
    const u32 f = ((lane & 15) >> 2) | ((g & 1) << 2);
    const u32 tr0 = L + (8 * g + ((lane & 15) >> 2)) * 256 + 32 * ((2 * w) ^ f) + 8 * (lane & 3);
    const u32 tr1 = tr0 ^ 32;
    const u32 kr0 = L + (16 * w + c) * 256 + ((g ^ c) << 4);
    f32x4 acc0, acc1;

    auto csync = [&]() {
      waitlds();
      if (lane == 0) addctl(L + L_CTL + C_SYNC);
      nsync += 4;
      wait_ctl(L + L_CTL + C_SYNC, nsync, syn, budget);
    };
    auto gemv = [&](int t0, int t1, u32 ba) {
      acc0 = f32x4{0.f, 0.f, 0.f, 0.f}; acc1 = acc0;
      for (int t = t0; t < t1; t++, ba += 128) {
        wait_ctl(L + L_CTL + C_FULL, t + 1, full, budget);
        const u32 sb = (t & (NSLOT - 1)) * TILE;
        s16x4 v[8];
#pragma unroll
        for (int i = 0; i < 8; i++)
          v[i] = __builtin_amdgcn_ds_read_tr16_b64_v4i16((LAS s16x4*)(uintptr_t)(((i & 4) ? tr1 : tr0) + sb + ((i >> 1) & 1) * 8192 + (i & 1) * 1024));
        const u32x4 p0 = ldl<u32x4>(ba), p1 = ldl<u32x4>(ba + 64);
        waitlds();
        stctl(L + L_CTL + C_DONE + 4 * w, t + 1);
        u32x4 fr[4];
#pragma unroll
        for (int i = 0; i < 4; i++) {
          const u32x2 lo = __builtin_bit_cast(u32x2, v[2 * i]), hi = __builtin_bit_cast(u32x2, v[2 * i + 1]);
          fr[i] = u32x4{lo[0], lo[1], hi[0], hi[1]};
        }
        acc0 = mfma(fr[0], p0, acc0); acc1 = mfma(fr[2], p0, acc1);
        acc0 = mfma(fr[1], p1, acc0); acc1 = mfma(fr[3], p1, acc1);
      }
    };
    const int srcl = (16 * (lane & 3) + 8 * ((lane >> 2) & 1) + (lane >> 3)) * 4;
    auto publish = [&](u32 off, int id) {
      if (X_NOPUB & id) return;
      const f32x4 v = (lane & 8) ? acc1 : acc0;
      u32x4 r;
#pragma unroll
      for (int i = 0; i < 4; i++) r[i] = __builtin_amdgcn_ds_bpermute(srcl, ((f2u(v[i]) + (TMASK + 1) / 2) & ~TMASK) | (tag & TMASK));
      if (id == 8) __builtin_amdgcn_raw_buffer_store_b128(r, rw, off, 0, X_FPST);
      else __builtin_amdgcn_raw_buffer_store_b128(r, rw, off, 0, X_STAUX);
    };
    float mx;
    auto qk = [&](int t0, int t1, u32 qa, u32 sa, bool wr) {
      u32x4 q[4];
#pragma unroll
      for (int s = 0; s < 4; s++) q[s] = ldl<u32x4>(qa + 64 * s);
      mx = -3.0e38f;
      for (int t = t0; t < t1; t++, sa += 128) {
        wait_ctl(L + L_CTL + C_FULL, t + 1, full, budget);
        const u32 ka = kr0 + (t & (NSLOT - 1)) * TILE;
        u32x4 k[4];
#pragma unroll
        for (int s = 0; s < 4; s++) k[s] = ldl<u32x4>(ka ^ (s << 6));
        waitlds();
        stctl(L + L_CTL + C_DONE + 4 * w, t + 1);
        f32x4 d = {0.f, 0.f, 0.f, 0.f};
#pragma unroll
        for (int s = 0; s < 4; s++) d = mfma(k[s], q[s], d);
        constexpr float SC = 0.08838834764831845f;
        const u32 u01 = pack2(d[0], d[1]), u23 = pack2(d[2], d[3]);
        const float s0 = blo(u01) * SC, s1 = bhi(u01) * SC, s2 = blo(u23) * SC, s3 = bhi(u23) * SC;
        mx = fmaxf(fmaxf(mx, fmaxf(s0, s1)), fmaxf(s2, s3));
        if (wr) stl<u32x2>(sa, u32x2{pack2(s0, s1), pack2(s2, s3)});
      }
      mx = rbf(x32_max(x16_max(mx)));
    };
    constexpr float LOG2E = 1.4426950408889634f;
    const u32 colofs = 128 * w + (lane & 7) * 16, pc = lane >> 3;
    const u32 xop = (lane & 7) * RS + 16 * g;

    gemv(0, 2, L + L_X + xop);
    publish(W_QP + (pc * 32 + h) * 4096 + b * 512 + colofs, 1);
    if (lane == 0) addctl(L + L_CTL + C_PUB);
    STAMP(1);
    gemv(2, 4, L + L_X + xop);
    publish(W_QP + 256 * 4096 + (pc * 32 + h) * 4096 + b * 512 + colofs, 2);
    wait_ctl(L + L_CTL + C_RDY, 1, rdy, budget);
    STAMP(2);
    qk(T_AK, T_BK, L + L_QA + 16 * g, L + L_SA + (16 * w + 4 * g) * 2, c == 0);
    STAMP(3);
    if (lane == 0) stl<float>(L + L_RED + 4 * w, mx);
    csync();
    {
      const f32x4 m4 = ldl<f32x4>(L + L_RED);
      const float M = fmaxf(fmaxf(m4[0], m4[1]), fmaxf(m4[2], m4[3]));
      const u32 pa = L + L_SA + w * 2048 + lane * 16;
      const u32x4 s0 = ldl<u32x4>(pa), s1 = ldl<u32x4>(pa + 1024);
      float e[16], sum = 0.f;
#pragma unroll
      for (int i = 0; i < 4; i++) {
        e[2 * i] = __builtin_amdgcn_exp2f((blo(s0[i]) - M) * LOG2E); e[2 * i + 1] = __builtin_amdgcn_exp2f((bhi(s0[i]) - M) * LOG2E);
        e[8 + 2 * i] = __builtin_amdgcn_exp2f((blo(s1[i]) - M) * LOG2E); e[9 + 2 * i] = __builtin_amdgcn_exp2f((bhi(s1[i]) - M) * LOG2E);
      }
#pragma unroll
      for (int i = 0; i < 16; i++) sum += e[i];
      sum = x32_sum(x16_sum(sum16(sum)));
      if (lane == 0) stl<float>(L + L_RED + 16 + 4 * w, sum);
      csync();
      const f32x4 l4 = ldl<f32x4>(L + L_RED + 16);
      const float inv = 1.0f / ((l4[0] + l4[1]) + (l4[2] + l4[3]));
      u32x4 p0, p1;
#pragma unroll
      for (int i = 0; i < 4; i++) { p0[i] = pack2(e[2 * i] * inv, e[2 * i + 1] * inv); p1[i] = pack2(e[8 + 2 * i] * inv, e[9 + 2 * i] * inv); }
      stl<u32x4>(pa, p0); stl<u32x4>(pa + 1024, p1);
    }
    STAMP(4);
    wait_ctl(L + L_CTL + C_RDY, 2, rdy, budget);
    qk(T_BK, T_BV, L + L_QB + xop, L + L_SB + (lane & 7) * SBS + (16 * w + 4 * g) * 2, c < 8);
    if (lane < 8) stl<float>(L + L_RED + 64 + lane * 16 + 4 * w, mx);
    csync();
    {
      const int hh = 2 * w + (lane >> 5);
      const f32x4 m4 = ldl<f32x4>(L + L_RED + 64 + hh * 16);
      const float M = fmaxf(fmaxf(m4[0], m4[1]), fmaxf(m4[2], m4[3]));
      const u32 pa = L + L_SB + hh * SBS + (lane & 31) * 16;
      const u32x4 s0 = ldl<u32x4>(pa), s1 = ldl<u32x4>(pa + 512);
      float sum = 0.f;
      u32x4 p0, p1;
#pragma unroll
      for (int i = 0; i < 4; i++) {
        const float e0 = __builtin_amdgcn_exp2f((blo(s0[i]) - M) * LOG2E), e1 = __builtin_amdgcn_exp2f((bhi(s0[i]) - M) * LOG2E);
        const float e2 = __builtin_amdgcn_exp2f((blo(s1[i]) - M) * LOG2E), e3 = __builtin_amdgcn_exp2f((bhi(s1[i]) - M) * LOG2E);
        sum += (e0 + e1) + (e2 + e3);
        p0[i] = pack2(e0, e1); p1[i] = pack2(e2, e3);
      }
      stl<u32x4>(pa, p0); stl<u32x4>(pa + 512, p1);
      sum = x16_sum(sum16(sum));
      if ((lane & 31) == 0)
        __builtin_amdgcn_raw_buffer_store_b128(u32x4{f2u(M), tag, f2u(sum), tag}, rw, W_ML + ((cu & ~7) + hh) * 128 + (h & 7) * 16, 0, X_SMALLAUX);
    }
    csync();
    gemv(T_BV, T_AV, L + L_SB + (lane & 7) * SBS + 16 * g);
    publish(W_BP + ((cu & ~7) + pc) * 4096 + (h & 7) * 512 + colofs, 4);
    STAMP(5);
    gemv(T_AV, T_WO, L + L_SA + 16 * g);
    STAMP(6);
    {
      const f32x4 v = (lane & 8) ? acc1 : acc0;
      const u32 r0 = __builtin_amdgcn_ds_bpermute(srcl, pack2(v[0], v[1])), r1 = __builtin_amdgcn_ds_bpermute(srcl, pack2(v[2], v[3]));
      if (lane < 8) {
        __builtin_amdgcn_raw_buffer_store_b128(u32x4{r0, tag, r1, tag}, rw, W_OA + (h * 8 + b) * 512 + 128 * w + 16 * lane, 0, X_FPST);
        stl<u32x2>(L + L_OA + b * RS + 64 * w + 8 * lane, u32x2{r0, r1});
      }
      waitlds();
    }
    if (lane == 0) addctl(L + L_CTL + C_PUB);
    wait_ctl(L + L_CTL + C_RDY, 3, rdy, budget);
    gemv(T_WO, T_WO + 2, L + L_OB + xop);
    const f32x2 gg = ldl<f32x2>(L + L_G + (lane & 7) * 8);
    const f32x4 b0 = acc0 * gg[1], b1 = acc1 * gg[1];
    wait_ctl(L + L_CTL + C_RDY, 4, rdy, budget);
    STAMP(7);
    gemv(T_WO + 2, T_END, L + L_OA + xop);
    acc0 = acc0 * gg[0] + b0; acc1 = acc1 * gg[0] + b1;
    publish(W_FP + (pc * 32 + 4 * b + w) * 4096 + h * 128 + (lane & 7) * 16, 8);
    if (lane == 0) addctl(L + L_CTL + C_PUB);
    STAMP(8);
    STAMP_DUMP();
    return;
  }

  {
    STAMP(0);
    const u32x4 xa = *(const u32x4*)(a.x + b * 1024 + lane * 8), xb = *(const u32x4*)(a.x + b * 1024 + 512 + lane * 8);
    const u32x4 ga = *(const u32x4*)(a.gw + lane * 8), gb = *(const u32x4*)(a.gw + 512 + lane * 8);
    const u32 tag = __builtin_amdgcn_readfirstlane(tagraw) + 1u;
    int full = 0, pub = 0, budget = LCAP, gbudget = GCAP;
    u32x4 d[5]; int npass = 0;
    auto gatherp = [&](u32 off, int id) {
      if (X_NOGAT & id) { for (int i = 0; i < 4; i++) d[i] = u32x4{0x3c003c00u, 0, 0x3c003c00u, 0}; return; }
      for (;;) {
#pragma unroll
        for (int i = 0; i < 4; i++) d[i] = id == 8 ? __builtin_amdgcn_raw_buffer_load_b128(rw, lane * 16 + i * 1024, off, X_FPLD) : __builtin_amdgcn_raw_buffer_load_b128(rw, lane * 16 + i * 1024, off, X_LDAUX);
        u32 bad = 0;
#pragma unroll
        for (int i = 0; i < 4; i++) bad |= ((d[i][0] ^ tag) | (d[i][1] ^ tag)) | ((d[i][2] ^ tag) | (d[i][3] ^ tag));
        npass++;
        if (__all((bad & TMASK) == 0) || (X_NOPUB & id)) break;
        if (--gbudget < 0) break;
        __builtin_amdgcn_s_sleep(2);
        asm volatile("" ::: "memory");
      }
#pragma unroll
      for (int i = 0; i < 4; i++) d[i] &= ~TMASK;
    };
    auto try4 = [&](u32 off, int skip, bool extra, u32 eoff, bool fin) -> bool {
      bool ok = true;
#pragma unroll
      for (int i = 0; i < 4; i++) {
        const int vo = (2 * i + (lane >> 5) == skip) ? 0x7ff00000 : lane * 16 + i * 1024;
        d[i] = fin ? __builtin_amdgcn_raw_buffer_load_b128(rw, vo, off, X_FPLD) : __builtin_amdgcn_raw_buffer_load_b128(rw, vo, off, X_SMALLAUX);
      }
      if (extra) d[4] = __builtin_amdgcn_raw_buffer_load_b128(rw, (lane & 7) * 16, eoff, X_SMALLAUX);
#pragma unroll
      for (int i = 0; i < 4; i++) ok &= ((d[i][1] == tag) & (d[i][3] == tag)) | (2 * i + (lane >> 5) == skip);
      if (extra) ok &= (d[4][1] == tag) & (d[4][3] == tag);
      npass++;
      return __all(ok);
    };
    auto gather4 = [&](u32 off, int skip, bool extra, u32 eoff, bool fin) {
      while (!try4(off, skip, extra, eoff, fin)) {
        if (--gbudget < 0) break;
        __builtin_amdgcn_s_sleep(2);
        asm volatile("" ::: "memory");
      }
    };
    auto image4 = [&](u32 img, int skip) {
#pragma unroll
      for (int i = 0; i < 4; i++)
        if (2 * i + (lane >> 5) != skip) stl<u32x2>(img + (2 * i + (lane >> 5)) * RS + (lane & 31) * 8, u32x2{d[i][0], d[i][2]});
    };
    f32x4 s;
    auto sum4 = [&]() {
#pragma unroll
      for (int k = 0; k < 4; k++) s[k] = (u2f(d[0][k]) + u2f(d[1][k])) + (u2f(d[2][k]) + u2f(d[3][k]));
    };
    float z = 0.f;
#pragma unroll
    for (int i = 0; i < 4; i++) {
      const u32 pa = pack2(blo(xa[i]) * blo(ga[i]), bhi(xa[i]) * bhi(ga[i])), pb = pack2(blo(xb[i]) * blo(gb[i]), bhi(xb[i]) * bhi(gb[i]));
      z += (blo(pa) + bhi(pa)) + (blo(pb) + bhi(pb));
    }
    z = rbf(x32_sum(x16_sum(sum16(z))));
    const float gate = rbf(1.0f / (1.0f + __expf(-z))), gate1 = rbf(1.0f - gate);

    wait_ctl(L + L_CTL + C_FULL, QTRIG, full, budget);
    STAMP(12);
    gatherp(W_QP + cu * 4096, 1);
    STAMPV(8, npass); npass = 0;
    sum4();
#pragma unroll
    for (int k = 0; k < 4; k++) s[k] = x32_sum(s[k]);
    stl<u32x2>(L + L_QA + (lane & 31) * 8, u32x2{pack2(s[0], s[1]), pack2(s[2], s[3])});
    waitlds();
    stctl(L + L_CTL + C_RDY, 1);
    STAMP(1);
    wait_ctl(L + L_CTL + C_FULL, 14, full, budget);
    gatherp(W_QP + 256 * 4096 + cu * 4096, 2);
    STAMPV(9, npass); npass = 0;
    sum4();
#pragma unroll
    for (int k = 0; k < 4; k++) s[k] = x32_sum(s[k]);
    if (lane < 32) __builtin_amdgcn_raw_buffer_store_b128(u32x4{pack2(s[0], s[1]), tag, pack2(s[2], s[3]), tag}, rw, lane * 16, W_QF + cu * 512, X_SMALLAUX);
    wait_ctl(L + L_CTL + C_FULL, 40, full, budget);
    gather4(W_QF + (cu & ~7) * 512, -1, false, 0, false);
    image4(L + L_QB, -1);
    waitlds();
    stctl(L + L_CTL + C_RDY, 2);
    STAMP(2);
    wait_ctl(L + L_CTL + C_FULL, T_AV + 16, full, budget);
    {
      u32x4 ml;
      for (;;) {
        ml = __builtin_amdgcn_raw_buffer_load_b128(rw, (lane & 7) * 16, W_ML + cu * 128, X_SMALLAUX);
        if (__all((ml[1] == tag) & (ml[3] == tag))) break;
        if (--gbudget < 0) break;
        __builtin_amdgcn_s_sleep(2);
        asm volatile("" ::: "memory");
      }
      npass = 0;
      gatherp(W_BP + cu * 4096, 4);
      STAMPV(10, npass); npass = 0;
      float M = u2f(ml[0]);
      M = fmaxf(M, dpp<XOR1>(M)); M = fmaxf(M, dpp<XOR2>(M)); M = fmaxf(M, dpp<XOR7>(M));
      const float wt = __builtin_amdgcn_exp2f((u2f(ml[0]) - M) * 1.4426950408889634f);
      float Ls = wt * u2f(ml[2]);
      Ls += dpp<XOR1>(Ls); Ls += dpp<XOR2>(Ls); Ls += dpp<XOR7>(Ls);
      const float wn = wt / Ls;
      s = f32x4{0.f, 0.f, 0.f, 0.f};
#pragma unroll
      for (int i = 0; i < 4; i++) {
        const float w0 = u2f(__builtin_amdgcn_readlane(f2u(wn), 2 * i)), w1 = u2f(__builtin_amdgcn_readlane(f2u(wn), 2 * i + 1));
        const float wi = lane < 32 ? w0 : w1;
#pragma unroll
        for (int k = 0; k < 4; k++) s[k] = fmaf(wi, u2f(d[i][k]), s[k]);
      }
#pragma unroll
      for (int k = 0; k < 4; k++) s[k] = x32_sum(s[k]);
      if (lane < 32) __builtin_amdgcn_raw_buffer_store_b128(u32x4{pack2(s[0], s[1]), tag, pack2(s[2], s[3]), tag}, rw, lane * 16, W_OB + (h * 8 + b) * 512, X_SMALLAUX);
      if (lane == 32) __builtin_amdgcn_raw_buffer_store_b128(u32x4{f2u(gate), tag, f2u(gate1), tag}, rw, 0, W_G + h * 128 + b * 16, X_SMALLAUX);
    }
    wait_ctl(L + L_CTL + C_FULL, T_AV + 40, full, budget);
    gather4(W_OB + h * 4096, -1, true, W_G + h * 128, false);
    image4(L + L_OB, -1);
    stl<u32x2>(L + L_G + (lane & 7) * 8, u32x2{d[4][0], d[4][2]});
    waitlds();
    stctl(L + L_CTL + C_RDY, 3);
    STAMP(3);
    wait_ctl(L + L_CTL + C_PUB, 8, pub, budget);
    STAMP(4);
    npass = 0;
    if (!try4(W_OA + h * 4096, b, false, 0, true)) {
      wait_ctl(L + L_CTL + C_FULL, T_END, full, budget);
      gather4(W_OA + h * 4096, b, false, 0, true);
    }
    image4(L + L_OA, b);
    waitlds();
    stctl(L + L_CTL + C_RDY, 4);
    STAMP(5);
    STAMPV(13, npass); npass = 0;
    wait_ctl(L + L_CTL + C_PUB, 12, pub, budget);
    STAMP(6);
    gatherp(W_FP + cu * 4096, 8);
    STAMPV(11, npass); npass = 0;
    sum4();
#pragma unroll
    for (int k = 0; k < 4; k++) { s[k] += dpp<0x128>(s[k]); s[k] = x32_sum(x16_sum(s[k])); }
    if (lane < 8) *(u32x2*)(a.out + b * 1024 + h * 32 + lane * 4) = u32x2{pack2(s[0], s[1]), pack2(s[2], s[3])};
    if (wg == 0 && lane == 0) __builtin_amdgcn_raw_buffer_store_b32(tag, rw, 0, W_E, 16);
    STAMP(7);
    STAMP_DUMP();
  }
}

extern "C" void kernel_launch(void* const* d_in, const int* in_sizes, int n_in, void* d_out, int out_size, void* d_ws, size_t ws_size,
                              hipStream_t stream) {
  Args a;
  a.ph[0] = (const char*)d_in[3];
  a.ph[1] = (const char*)d_in[8];
  a.ph[2] = (const char*)d_in[1];
  a.ph[3] = (const char*)d_in[6];
  a.ph[4] = (const char*)d_in[7];
  a.ph[5] = (const char*)d_in[2];
  a.ph[6] = (const char*)d_in[9];
  a.ph[7] = (const char*)d_in[4];
  a.x = (const u16*)d_in[0];
  a.gw = (const u16*)d_in[10];
  a.out = (u16*)d_out;
  a.ws = (char*)d_ws;
  fused_decode<<<256, 384, 0, stream>>>(a);
}
```

```cpp
#include <hip/hip_runtime.h>
#include <stdint.h>

typedef unsigned int u32;
typedef unsigned short u16;
typedef u32 u32x2 __attribute__((ext_vector_type(2)));
typedef u32 u32x4 __attribute__((ext_vector_type(4)));
typedef float f32x2 __attribute__((ext_vector_type(2)));
typedef float f32x4 __attribute__((ext_vector_type(4)));
typedef short s16x4 __attribute__((ext_vector_type(4)));
typedef __bf16 bf16x2 __attribute__((ext_vector_type(2)));
typedef __bf16 bf16x8 __attribute__((ext_vector_type(8)));
typedef __amdgpu_buffer_rsrc_t rsrc_t;
#define LAS __attribute__((address_space(3)))
#define DEVI __device__ __forceinline__

constexpr int TILE = 16384, NSLOT = 8;
constexpr int L_SA = NSLOT * TILE;
constexpr int SBS = 1056;
constexpr int L_SB = L_SA + 8192;
constexpr int RS = 288;
constexpr int L_QA = L_SB + 8 * SBS;
constexpr int L_X = L_QA + 256;
constexpr int L_QB = L_X + 8 * RS;
constexpr int L_OB = L_QB + 8 * RS;
constexpr int L_OA = L_OB + 8 * RS;
constexpr int L_G = L_OA + 8 * RS;
constexpr int L_RED = L_G + 64;
constexpr int L_ML = L_RED + 320;
constexpr int L_DUMMY = L_ML + 64;
constexpr int L_CTL = L_DUMMY + 256;
constexpr int L_ST = L_CTL + 64;
constexpr int NWAVE = 7, LDS_BYTES = L_ST;
constexpr int C_DONE = 0, C_FULL = 16, C_POS = 20, C_RDY = 24, C_PUB = 28, C_SYNC = 32, C_ANC = 36;

constexpr u32 W_E = 0;
constexpr u32 W_QP = 4096;
constexpr u32 W_BP = W_QP + 2 * 256 * 4096;
constexpr u32 W_FP = W_BP + 256 * 4096;
constexpr u32 W_QF = W_FP + 256 * 4096;
constexpr u32 W_OB = W_QF + 256 * 512;
constexpr u32 W_OA = W_OB + 256 * 512;
constexpr u32 W_ML = W_OA + 256 * 512;
constexpr u32 W_TS = W_ML + 256 * 128;
constexpr u32 W_END = W_TS + 40 * 512;
constexpr u32 TMASK = 0xffu;
constexpr u32 OOB = 0x7ff00000u;
constexpr int NB = 8, DM = 1024;

constexpr int T_AK = 4, T_BK = 68, T_BV = 76, T_AV = 84, T_WO = 148, T_END = 152;

struct Seg {
  const char* ptr;
  u32 mulb, mulh;
  u32 nrec;
  u32 row;
  u32 thstop;
  u32 pq;
};
constexpr int NSEG = 10;
struct Args {
  const char* wqa;
  const u16* x;
  const u16* gw;
  u16* out;
  char* ws;
  int anc0;
  int setp;
  int ck0, ckstep, cklag, cklast;
  Seg seg[NSEG];
  int trig[8];
};
static_assert(sizeof(Seg) == 32 && sizeof(Args) == 64 + 32 * NSEG + 32, "no padding");

DEVI rsrc_t mk(const void* p, u32 n) { return __builtin_amdgcn_make_buffer_rsrc((void*)p, 0, n, 0x00020000); }
DEVI u32 pack2(float a, float b) { f32x2 v = {a, b}; return __builtin_bit_cast(u32, __builtin_convertvector(v, bf16x2)); }
DEVI float blo(u32 u) { return __builtin_bit_cast(float, u << 16); }
DEVI float bhi(u32 u) { return __builtin_bit_cast(float, u & 0xffff0000u); }
DEVI float rbf(float a) { return blo(pack2(a, a)); }
DEVI float u2f(u32 u) { return __builtin_bit_cast(float, u); }
DEVI u32 f2u(float f) { return __builtin_bit_cast(u32, f); }
DEVI u32 tagf(float v, u32 t8) { return ((f2u(v) + (TMASK + 1) / 2) & ~TMASK) | t8; }
DEVI f32x4 mfma(u32x4 a, u32x4 b, f32x4 c) {
  return __builtin_amdgcn_mfma_f32_16x16x32_bf16(__builtin_bit_cast(bf16x8, a), __builtin_bit_cast(bf16x8, b), c, 0, 0, 0);
}
template <int CTRL> DEVI float dpp(float x) { return u2f(__builtin_amdgcn_mov_dpp(f2u(x), CTRL, 0xf, 0xf, true)); }
constexpr int XOR1 = 0xB1, XOR2 = 0x4E, XOR7 = 0x141, XOR15 = 0x140, XOR8 = 0x128;
DEVI float sum16(float x) { x += dpp<XOR1>(x); x += dpp<XOR2>(x); x += dpp<XOR7>(x); x += dpp<XOR15>(x); return x; }
DEVI float x16_sum(float x) { auto s = __builtin_amdgcn_permlane16_swap(f2u(x), f2u(x), false, false); return u2f(s[0]) + u2f(s[1]); }
DEVI float x32_sum(float x) { auto s = __builtin_amdgcn_permlane32_swap(f2u(x), f2u(x), false, false); return u2f(s[0]) + u2f(s[1]); }
DEVI float x16_max(float x) { auto s = __builtin_amdgcn_permlane16_swap(f2u(x), f2u(x), false, false); return fmaxf(u2f(s[0]), u2f(s[1])); }
DEVI float x32_max(float x) { auto s = __builtin_amdgcn_permlane32_swap(f2u(x), f2u(x), false, false); return fmaxf(u2f(s[0]), u2f(s[1])); }
DEVI void waitlds() { asm volatile("s_waitcnt lgkmcnt(0)" ::: "memory"); }

template <typename T> DEVI T ldl(u32 a) { return *(const LAS T*)(uintptr_t)a; }
template <typename T> DEVI void stl(u32 a, T v) { *(LAS T*)(uintptr_t)a = v; }
DEVI void stctl(u32 a, int v) { *(volatile LAS int*)(uintptr_t)a = v; }
DEVI void addctl(u32 a) { asm volatile("ds_add_u32 %0, %1" ::"v"(a), "v"(1) : "memory"); }

constexpr int LCAP = 1 << 16, GCAP = 1 << 12;

template <int OA, int OB, int SLEEP>
DEVI void wait2(u32 ctl, int wa, int wb, int& sa, int& sb, int& budget) {
  if (sa < wa || sb < wb) {
    int t0, t1;
    asm volatile(
        "s_branch 2f\n"
        "1:\n\t"
        ".if %c[sl]\n\ts_sleep %c[sl]\n\t.endif\n"
        "2:\n\t"
        "ds_read_b32 %[t0], %[c] offset:%c[oa]\n\t"
        "ds_read_b32 %[t1], %[c] offset:%c[ob]\n\t"
        "s_sub_i32 %[bu], %[bu], 1\n\t"
        "s_cmp_lt_i32 %[bu], 0\n\t"
        "s_waitcnt lgkmcnt(0)\n\t"
        "v_readfirstlane_b32 %[sa], %[t0]\n\t"
        "v_readfirstlane_b32 %[sb], %[t1]\n\t"
        "s_cbranch_scc1 3f\n\t"
        "s_cmp_lt_i32 %[sa], %[wa]\n\t"
        "s_cbranch_scc1 1b\n\t"
        "s_cmp_lt_i32 %[sb], %[wb]\n\t"
        "s_cbranch_scc1 1b\n"
        "3:"
        : [t0] "=&v"(t0), [t1] "=&v"(t1), [sa] "=&s"(sa), [sb] "=&s"(sb), [bu] "+s"(budget)
        : [c] "v"(ctl), [wa] "s"(wa), [wb] "s"(wb), [oa] "i"(OA), [ob] "i"(OB), [sl] "i"(SLEEP)
        : "memory", "scc");
  }
  asm volatile("" ::: "memory");
}

#define STAMPV(i, v) do { } while (0)
#define STAMP(i) do { } while (0)
#define STAMP_INIT() do { } while (0)
#define STAMP_DUMP() do { } while (0)
#define SLP 1

template <int INF, int INFH>
__global__ void __launch_bounds__(NWAVE * 64, 2) fused_decode(Args a) {
  __shared__ __attribute__((aligned(1024))) char lds[LDS_BYTES];
  const u32 L = (u32)(uintptr_t)(LAS char*)lds;
  const int wave = __builtin_amdgcn_readfirstlane(threadIdx.x >> 6), lane = threadIdx.x & 63;
  const int wg = blockIdx.x, h = (wg & 7) * 4 + ((wg >> 3) & 3), b = wg >> 5, cu = b * 32 + h;
  const int g = lane >> 4, c = lane & 15;

  {
    const int ini = c < 4 ? 4 * NSLOT : 0;
    const int xv0 = (c ^ (g << 1)) << 4;
    int n, so, tmp, tg, st, stop, bud, ko;
    unsigned long long t_end;
#define LD(j, o) "buffer_load_dwordx4 v" #j ", s[72:75], %[so] offen offset:" #o " nt lds\n\t"
#define WT(W) "s_waitcnt vmcnt(%c[" #W "])\n\t"
#define PUB(W, j) ".if (%c[" #W "] & 3) == " #j "\n\tds_write_b64 %[ctl], v[44:45] offset:%c[cf]\n\t.endif\n\t"
#define GROUP(W, k) \
        "s_sub_i32 %[tmp], %[n], %c[" #W "] >> 2\n\t" \
        "v_max_i32 v44, %[tmp], v44\n\t" \
        WT(W) LD(40, 0) PUB(W, 0) "ds_read_b32 v46, %[dn]\n\t" "ds_read_b32 v47, %[ctl] offset:%c[ca]\n\t" \
        WT(W) LD(41, 1024) PUB(W, 1) "s_add_i32 %[n], %[n], 1\n\t" "s_add_u32 %[tg], %[tg], s77\n\t" \
        WT(W) LD(42, 2048) PUB(W, 2) "s_memrealtime s[60:61]\n\t" "s_lshl_b32 %[tmp], %[n], 12\n\t" \
        WT(W) LD(43, 3072) PUB(W, 3) "s_and_b32 m0, %[tmp], 0x1f000\n\t" "s_add_u32 %[so], %[so], %[st]\n\t" \
        "s_waitcnt lgkmcnt(0)\n\t" \
        "v_readfirstlane_b32 s62, v47\n\t" \
        "v_cmp_ge_i32 vcc, %[n], v46\n" \
        "2" #k ":\n\t" \
        "s_lshl_b32 s63, s60, 8\n\t" \
        "s_sub_u32 s63, s63, %[tg]\n\t" \
        "s_sub_u32 %[tmp], s63, s62\n\t" \
        "s_cmp_ge_i32 %[tmp], 0\n\t" \
        "s_cbranch_scc1 3" #k "f\n\t" \
        "s_memrealtime s[60:61]\n\t" \
        "s_waitcnt lgkmcnt(0)\n\t" \
        "s_branch 2" #k "b\n" \
        "3" #k ":\n\t" \
        "v_mov_b32 v45, s63\n\t" \
        "s_cbranch_vccnz 7f\n\t" \
        "s_cmp_ge_i32 %[n], %[stop]\n\t" \
        "s_cbranch_scc1 5b\n\t" \
        "s_cmp_le_i32 %[tmp], s76\n\t"
    asm volatile(
        "s_load_dwordx2 s[72:73], %[ka], 0x0\n\t"
        "s_load_dwordx8 s[64:71], %[ka], 0x40\n\t"
        "s_load_dword s62, %[ka], 0x28\n\t"
        "s_memrealtime s[60:61]\n\t"
        "ds_write_b32 %[ia], %[iv]\n\t"
        "v_lshl_add_u32 v40, %[g], 13, %[xv0]\n\t"
        "v_xor_b32 v42, 0x80, v40\n\t"
        "v_add_u32 v41, 0x7c00, v40\n\t"
        "v_add_u32 v43, 0x17400, v42\n\t"
        "v_add_u32 v42, 0xf800, v42\n\t"
        "s_lshl_b32 %[tmp], %[b], 20\n\t"
        "s_lshl_b32 %[so], %[h], 8\n\t"
        "s_add_u32 %[tmp], %[tmp], %[so]\n\t"
        "s_mov_b32 s74, 0xfe100\n\t"
        "s_mov_b32 s75, 0x20000\n\t"
        "s_mov_b32 m0, 0\n\t"
        "s_mov_b32 %[so], 0\n\t"
        "s_cmp_lg_u32 %[wv], 0\n\t"
        "s_waitcnt lgkmcnt(0)\n\t"
        "s_barrier\n\t"
        "s_cbranch_scc1 99f\n\t"
        "s_add_u32 s72, s72, %[tmp]\n\t"
        "s_addc_u32 s73, s73, 0\n"
        "1:\n\t"
        LD(40, 0) LD(41, 1024) LD(42, 2048) LD(43, 3072)
        "s_add_u32 m0, m0, 0x1000\n\t"
        "s_add_u32 %[so], %[so], 0x20000\n\t"
        "s_cmp_lt_u32 %[so], 0x100000\n\t"
        "s_cbranch_scc1 1b\n\t"
        "s_setprio 3\n\t"
        "s_lshl_b32 s63, s60, 8\n\t"
        "s_add_u32 s62, s62, s63\n\t"
        "v_mov_b32 v47, s62\n\t"
        "v_mov_b32 v44, 0\n\t"
        "v_mov_b32 v45, 0\n\t"
        "ds_write_b32 %[ctl], v47 offset:%c[ca]\n\t"
        "s_mov_b32 %[n], 8\n\t"
        "s_mov_b32 %[tg], 0\n\t"
        "s_mov_b32 %[bud], 0x10000\n\t"
        "s_mov_b32 %[ko], 0x40\n"
        "5:\n\t"
        "s_cmp_ge_i32 %[n], %c[nend]\n\t"
        "s_cbranch_scc1 8f\n\t"
        "s_mul_i32 %[tmp], %[b], s66\n\t"
        "s_lshr_b32 %[so], s67, 16\n\t"
        "s_mul_i32 %[so], %[so], %[hh]\n\t"
        "s_add_u32 %[tmp], %[tmp], %[so]\n\t"
        "s_and_b32 %[so], s67, 0xffff\n\t"
        "s_mul_i32 %[so], %[so], %[hl]\n\t"
        "s_add_u32 %[tmp], %[tmp], %[so]\n\t"
        "s_add_u32 s72, s64, %[tmp]\n\t"
        "s_addc_u32 s73, s65, 0\n\t"
        "s_mov_b32 s74, s68\n\t"
        "s_mov_b64 s[76:77], s[70:71]\n\t"
        "s_and_b32 %[stop], s70, 0x3ff\n\t"
        "s_and_b32 %[tmp], s69, 0xffff\n\t"
        "s_bfe_u32 %[st], s69, 0xf0010\n\t"
        "s_lshl_b32 %[st], %[st], 8\n\t"
        "s_add_u32 %[ko], %[ko], 32\n\t"
        "s_bitcmp1_b32 s69, 31\n\t"
        "s_cselect_b64 vcc, -1, 0\n\t"
        "s_load_dwordx8 s[64:71], %[ka], %[ko]\n\t"
        "s_mov_b32 %[so], 0\n\t"
        "v_cndmask_b32 v40, %[xv0], %[xk0], vcc\n\t"
        "v_cndmask_b32 v41, %[xv1], %[xk1], vcc\n\t"
        "v_cndmask_b32 v42, %[xv2], %[xk2], vcc\n\t"
        "v_cndmask_b32 v43, %[xv3], %[xk3], vcc\n\t"
        "v_mad_u32_u24 v40, %[g], %[tmp], v40\n\t"
        "v_mad_u32_u24 v41, %[g1], %[tmp], v41\n\t"
        "v_mad_u32_u24 v42, %[g2], %[tmp], v42\n\t"
        "v_mad_u32_u24 v43, %[g3], %[tmp], v43\n"
        "10:\n\t" GROUP(wl, 0)
        "s_cbranch_scc1 10b\n"
        "11:\n\t" GROUP(wh, 1)
        "s_cbranch_scc1 10b\n\t"
        "s_branch 11b\n"
        "7:\n\t"
        "ds_read_b32 v46, %[dn]\n\t"
        "s_sub_i32 %[bud], %[bud], 1\n\t"
        "s_waitcnt lgkmcnt(0)\n\t"
        "v_cmp_ge_i32 vcc, %[n], v46\n\t"
        "s_cmp_lt_i32 %[bud], 0\n\t"
        "s_cbranch_scc1 6f\n\t"
        "s_cbranch_vccz 6f\n\t"
        "s_waitcnt vmcnt(0)\n\t"
        "v_mov_b32 v44, %[n]\n\t"
        "ds_write_b32 %[ctl], v44 offset:%c[cf]\n\t"
        "s_sleep 1\n\t"
        "s_branch 7b\n"
        "6:\n\t"
        "s_cmp_ge_i32 %[n], %[stop]\n\t"
        "s_cbranch_scc1 5b\n\t"
        "s_branch 10b\n"
        "8:\n\t"
        "v_max_i32 v44, %c[nend] - 4, v44\n\t"
        "s_waitcnt vmcnt(16)\n\t"
        "ds_write_b32 %[ctl], v44 offset:%c[cf]\n\t"
        "v_mov_b32 v46, %c[nend]\n\t"
        "s_waitcnt vmcnt(0)\n\t"
        "ds_write_b32 %[ctl], v46 offset:%c[cf]\n\t"
        "s_endpgm\n"
        "99:\n\t"
        "s_mov_b64 %[te], s[60:61]"
        : [n] "=&s"(n), [so] "=&s"(so), [tmp] "=&s"(tmp), [tg] "=&s"(tg), [st] "=&s"(st), [stop] "=&s"(stop), [bud] "=&s"(bud), [ko] "=&s"(ko), [te] "=&s"(t_end)
        : [ka] "s"(__builtin_amdgcn_kernarg_segment_ptr()), [wv] "s"(wave), [b] "s"(b), [h] "s"(h), [hh] "s"((h >> 3) << 8), [hl] "s"((h & 7) << 8),
          [ia] "v"(L + L_CTL + 4 * c), [iv] "v"(ini), [ctl] "v"(L + L_CTL), [dn] "v"(L + L_CTL + C_DONE + 4 * (lane & 3)),
          [g] "v"(g), [g1] "v"(g + 4), [g2] "v"(g + 8), [g3] "v"(g + 12),
          [xv0] "v"(xv0), [xv1] "v"(xv0 - 1024), [xv2] "v"((xv0 ^ 128) - 2048), [xv3] "v"((xv0 ^ 128) - 3072),
          [xk0] "v"((c ^ g) << 4), [xk1] "v"(((c ^ g ^ 4) << 4) - 1024), [xk2] "v"(((c ^ g ^ 8) << 4) - 2048), [xk3] "v"(((c ^ g ^ 12) << 4) - 3072),
          [wl] "i"(INF - 1), [wh] "i"(INFH - 1), [cf] "i"(C_FULL), [ca] "i"(C_ANC), [nend] "i"(4 * T_END)
        : "memory", "scc", "vcc", "s60", "s61", "s62", "s63", "s64", "s65", "s66", "s67", "s68", "s69", "s70", "s71", "s72", "s73", "s74", "s75", "s76", "s77",
          "v40", "v41", "v42", "v43", "v44", "v45", "v46", "v47");
  }

  const rsrc_t rw = mk(a.ws, W_END);
  const u32 tagraw = __builtin_amdgcn_raw_buffer_load_b32(rw, 0, W_E, 16);

  if (wave == 6) {
    const u32 tag = __builtin_amdgcn_readfirstlane(tagraw) + 1u;
    int full = 0, pos = 0, budget = LCAP, k = 0;
    for (int n = a.ck0; n <= a.cklast; n += a.ckstep, k++) {
      full = 0;
      wait2<C_FULL, C_POS, 4>(L + L_CTL, n, (int)0x80000000, full, pos, budget);
      const int own = pos;
      __builtin_amdgcn_raw_buffer_store_b64(u32x2{(u32)own, tag}, rw, (wg < 64 && lane == 0) ? wg * 8 : OOB, W_TS + k * 512, 16);
      wait2<C_FULL, C_POS, 4>(L + L_CTL, n + a.cklag, (int)0x80000000, full, pos, budget);
      const u32x2 r = __builtin_amdgcn_raw_buffer_load_b64(rw, lane * 8, W_TS + k * 512, 16);
      const int d = (int)(r[0] - (u32)own);
      const bool ok = r[1] == tag && d > -(800 << 8) && d < (800 << 8);
      const float cnt = (float)__builtin_popcountll(__ballot(ok));
      const float sum = x32_sum(x16_sum(sum16(ok ? (float)d : 0.f)));
      if (cnt >= 16.f || k == 0) {
        const int mean = cnt >= 16.f ? (int)(sum / cnt) : 0;
        stctl(L + L_CTL + C_ANC, own + mean - a.setp);
      }
    }
    STAMP_DUMP();
    return;
  }

  if (wave < 5) {
    const int w = wave - 1;
    STAMP(9);
    {
      const u32x4 x0 = *(const u32x4*)(a.x + g * 1024 + b * 128 + c * 8);
      const u32x4 x1 = *(const u32x4*)(a.x + (g + 4) * 1024 + b * 128 + c * 8);
      stl<u32x4>(L + L_X + g * RS + c * 16, x0);
      stl<u32x4>(L + L_X + (g + 4) * RS + c * 16, x1);
    }
    const u32 t8 = (__builtin_amdgcn_readfirstlane(tagraw) + 1u) & TMASK;
    int full = 0, rdy = 0, syn = 0, nsync = 0, budget = LCAP;
    const u32 f = ((lane & 15) >> 2) | ((g & 1) << 2);
    const u32 tr0 = L + (8 * g + ((lane & 15) >> 2)) * 256 + 32 * ((2 * w) ^ f) + 8 * (lane & 3);
    const u32 kr0 = L + (16 * w + c) * 256 + ((g ^ c) << 4);
    constexpr float LOG2E = 1.4426950408889634f;
    const u32 xop = (lane & 7) * RS + 16 * g;
    const int srcl = (16 * (lane & 3) + 8 * ((lane >> 2) & 1) + (lane >> 3)) * 4;
    const u32 pc = lane >> 3;
    auto csync = [&]() {
      waitlds();
      if (lane == 0) addctl(L + L_CTL + C_SYNC);
      nsync += 4;
      wait2<C_SYNC, C_RDY, 0>(L + L_CTL, nsync, 0, syn, rdy, budget);
    };
    f32x4 keep0 = {0.f, 0.f, 0.f, 0.f}, keep1 = keep0;
#pragma nounroll
    for (int job = 0; job < 6; job++) {
      if (job == 2) {
#pragma nounroll
        for (int br = 0; br < 2; br++) {
          wait2<C_FULL, C_RDY, SLP>(L + L_CTL, 0, br + 1, full, rdy, budget);
          STAMP(10 + br);
          const u32 qa = br ? L + L_QB + xop : L + L_QA + 16 * g;
          u32 sa = (br ? L + L_SB + (lane & 7) * SBS : L + L_SA) + (16 * w + 4 * g) * 2;
          const bool wr = c < (br ? 8 : 1);
          u32x4 q[4];
#pragma unroll
          for (int s = 0; s < 4; s++) q[s] = ldl<u32x4>(qa + 64 * s);
          float mx = -3.0e38f;
          const int t1 = br ? T_BV : T_BK;
          for (int t = br ? T_BK : T_AK; t < t1; t++, sa += 128) {
            wait2<C_FULL, C_RDY, SLP>(L + L_CTL, 4 * t + 4, 0, full, rdy, budget);
            const u32 ka = kr0 + (t & (NSLOT - 1)) * TILE;
            u32x4 k[4];
#pragma unroll
            for (int s = 0; s < 4; s++) k[s] = ldl<u32x4>(ka ^ (s << 6));
            waitlds();
            stctl(L + L_CTL + C_DONE + 4 * w, 4 * (t + 1 + NSLOT));
            f32x4 d = {0.f, 0.f, 0.f, 0.f};
#pragma unroll
            for (int s = 0; s < 4; s++) d = mfma(k[s], q[s], d);
            constexpr float SC = 0.08838834764831845f;
            const u32 u01 = pack2(d[0], d[1]), u23 = pack2(d[2], d[3]);
            const float s0 = blo(u01) * SC, s1 = bhi(u01) * SC, s2 = blo(u23) * SC, s3 = bhi(u23) * SC;
            mx = fmaxf(fmaxf(mx, fmaxf(s0, s1)), fmaxf(s2, s3));
            if (wr) stl<u32x2>(sa, u32x2{pack2(s0, s1), pack2(s2, s3)});
          }
          STAMP(12 + br);
          mx = rbf(x32_max(x16_max(mx)));
          if (lane < 8) stl<float>(L + L_RED + br * 128 + lane * 16 + 4 * w, mx);
          csync();
          const int hh = 2 * w + (lane >> 5);
          const f32x4 m4 = ldl<f32x4>(L + L_RED + br * 128 + hh * 16);
          const float M = fmaxf(fmaxf(m4[0], m4[1]), fmaxf(m4[2], m4[3]));
          const u32 pa = (br ? L + L_SB + hh * SBS : L + L_SA + hh * 1024) + (lane & 31) * 16;
          const u32x4 s0 = ldl<u32x4>(pa), s1 = ldl<u32x4>(pa + 512);
          float e[16], sum = 0.f;
#pragma unroll
          for (int i = 0; i < 4; i++) {
            e[2 * i] = __builtin_amdgcn_exp2f((blo(s0[i]) - M) * LOG2E); e[2 * i + 1] = __builtin_amdgcn_exp2f((bhi(s0[i]) - M) * LOG2E);
            e[8 + 2 * i] = __builtin_amdgcn_exp2f((blo(s1[i]) - M) * LOG2E); e[9 + 2 * i] = __builtin_amdgcn_exp2f((bhi(s1[i]) - M) * LOG2E);
          }
#pragma unroll
          for (int i = 0; i < 16; i++) sum += e[i];
          sum = x16_sum(sum16(sum));
          float inv = 1.0f;
          if (br == 0) {
            sum = x32_sum(sum);
            if (lane == 0) stl<float>(L + L_RED + 256 + 4 * w, sum);
            csync();
            const f32x4 l4 = ldl<f32x4>(L + L_RED + 256);
            inv = 1.0f / ((l4[0] + l4[1]) + (l4[2] + l4[3]));
          } else if ((lane & 31) == 0) {
            stl<u32x2>(L + L_ML + hh * 8, u32x2{tagf(M, t8), tagf(sum, t8)});
          }
          u32x4 p0, p1;
#pragma unroll
          for (int i = 0; i < 4; i++) { p0[i] = pack2(e[2 * i] * inv, e[2 * i + 1] * inv); p1[i] = pack2(e[8 + 2 * i] * inv, e[9 + 2 * i] * inv); }
          stl<u32x4>(pa, p0); stl<u32x4>(pa + 512, p1);
        }
        csync();
      }
      const int t0 = job < 2 ? 2 * job : job == 2 ? T_BV : job == 3 ? T_AV : T_WO + 2 * (job - 4);
      const int t1 = job == 2 ? T_AV : job == 3 ? T_WO : t0 + 2;
      u32 ba = L + (job < 2 ? L_X + xop : job == 2 ? L_SB + (lane & 7) * SBS + 16 * g : job == 3 ? L_SA + 16 * g : job == 4 ? L_OB + xop : L_OA + xop);
      u32 off = job < 2 ? W_QP + job * 256 * 4096 + (pc * 32 + h) * 4096 + b * 512 + 128 * w
              : job == 2 ? W_BP + ((cu & ~7) + pc) * 4096 + (h & 7) * 512 + 128 * w
              : job == 3 ? (pc == 0 ? W_OA + (h * 8 + b) * 512 + 128 * w : OOB)
                         : W_FP + (pc * 32 + 4 * b + w) * 4096 + h * 128;
      off += (lane & 7) * 16;
      u32 own = L + (job == 3 ? L_OA + b * RS : L_DUMMY) + 64 * w + 8 * (lane & 7);
      u32 ga = L + L_G + (lane & 7) * 8 + 4 * (job & 1);
      asm volatile("" : "+v"(off), "+v"(own), "+v"(ga));
      f32x4 acc0 = {0.f, 0.f, 0.f, 0.f}, acc1 = acc0;
      float gv = 0.f;
      for (int t = t0; t < t1; t += 2, ba += 256) {
        s16x4 v[16];
        u32x4 p[4];
        u32 a0 = tr0 + (t & (NSLOT - 1)) * TILE, a1 = a0 ^ 32;
        asm volatile("" : "+v"(a0), "+v"(a1));
        wait2<C_FULL, C_RDY, SLP>(L + L_CTL, 4 * t + 4, 0, full, rdy, budget);
#pragma unroll
        for (int i = 0; i < 8; i++)
          v[i] = __builtin_amdgcn_ds_read_tr16_b64_v4i16((LAS s16x4*)(uintptr_t)(((i & 4) ? a1 : a0) + ((i >> 1) & 1) * 8192 + (i & 1) * 1024));
        waitlds();
        stctl(L + L_CTL + C_DONE + 4 * w, 4 * (t + 1 + NSLOT));
        wait2<C_FULL, C_RDY, SLP>(L + L_CTL, 4 * t + 8, job - 1, full, rdy, budget);
#pragma unroll
        for (int i = 0; i < 4; i++) p[i] = ldl<u32x4>(ba + 64 * i);
#pragma unroll
        for (int i = 0; i < 8; i++)
          v[8 + i] = __builtin_amdgcn_ds_read_tr16_b64_v4i16((LAS s16x4*)(uintptr_t)(((i & 4) ? a1 : a0) + TILE + ((i >> 1) & 1) * 8192 + (i & 1) * 1024));
        gv = ldl<float>(ga);
        __builtin_amdgcn_sched_barrier(0);
#pragma unroll
        for (int i = 0; i < 4; i++) {
          const int j = 8 * (i >> 1) + 2 * (i & 1);
          const u32x2 a0 = __builtin_bit_cast(u32x2, v[j]), a1 = __builtin_bit_cast(u32x2, v[j + 1]);
          const u32x2 b0 = __builtin_bit_cast(u32x2, v[j + 4]), b1 = __builtin_bit_cast(u32x2, v[j + 5]);
          acc0 = mfma(u32x4{a0[0], a0[1], a1[0], a1[1]}, p[i], acc0);
          acc1 = mfma(u32x4{b0[0], b0[1], b1[0], b1[1]}, p[i], acc1);
        }
        asm volatile("" : "+v"(gv));
        waitlds();
        stctl(L + L_CTL + C_DONE + 4 * w, 4 * (t + 2 + NSLOT));
      }
      const float sc = job < 4 ? 1.0f : gv;
      acc0 = acc0 * sc + keep0; acc1 = acc1 * sc + keep1;
      if (job == 4) { keep0 = acc0; keep1 = acc1; STAMP(job); continue; }
      const f32x4 v = (lane & 8) ? acc1 : acc0;
      u32x4 r;
#pragma unroll
      for (int i = 0; i < 4; i++) r[i] = __builtin_amdgcn_ds_bpermute(srcl, tagf(v[i], t8));
      if ((a.trig[7] >> job) & 1) __builtin_amdgcn_raw_buffer_store_b128(r, rw, off, 0, 18);
      else __builtin_amdgcn_raw_buffer_store_b128(r, rw, off, 0, 16);
      if (job == 2 && w == 0 && lane < 8) {
        const u32x2 ml = ldl<u32x2>(L + L_ML + lane * 8);
        __builtin_amdgcn_raw_buffer_store_b128(u32x4{ml[0], ml[1], t8, t8}, rw, W_ML + cu * 128 + lane * 16, 0, 16);
      }
      r &= ~TMASK;
      stl<u32x2>(own, u32x2{pack2(u2f(r[0]), u2f(r[1])), pack2(u2f(r[2]), u2f(r[3]))});
      waitlds();
      if (lane == 0) addctl(L + L_CTL + C_PUB);
      STAMP(job);
    }
    STAMP_DUMP();
    return;
  }

  {
    STAMP(0);
    const u32x4 xa = *(const u32x4*)(a.x + b * 1024 + lane * 8), xb = *(const u32x4*)(a.x + b * 1024 + 512 + lane * 8);
    const u32x4 ga = *(const u32x4*)(a.gw + lane * 8), gb = *(const u32x4*)(a.gw + 512 + lane * 8);
    const u32 tag = __builtin_amdgcn_readfirstlane(tagraw) + 1u, t8 = tag & TMASK;
    int full = 0, pub = 0, budget = LCAP, gbudget = GCAP;
    float z = 0.f;
#pragma unroll
    for (int i = 0; i < 4; i++) {
      const u32 pa = pack2(blo(xa[i]) * blo(ga[i]), bhi(xa[i]) * bhi(ga[i])), pb = pack2(blo(xb[i]) * blo(gb[i]), bhi(xb[i]) * bhi(gb[i]));
      z += (blo(pa) + bhi(pa)) + (blo(pb) + bhi(pb));
    }
    z = rbf(x32_sum(x16_sum(sum16(z))));
    const float gate = rbf(1.0f / (1.0f + __expf(-z)));
    const rsrc_t ro = mk(a.out, NB * DM * 2);
#pragma nounroll
    for (int job = 0; job < 7; job++) {
      const int wfull = a.trig[job];
      const u32 off = job < 2 ? W_QP + job * 256 * 4096 + cu * 4096 : job == 2 ? W_QF + (cu & ~7) * 512 : job == 3 ? W_BP + cu * 4096
                    : job == 4 ? W_OB + h * 4096 : job == 5 ? W_OA + h * 4096 : W_FP + cu * 4096;
      const bool extra = job == 3;
      const u32 eoff = !extra ? OOB : W_ML + ((cu & ~7) + (lane & 7)) * 128 + (h & 7) * 16;
      const int skip = job == 5 ? b : -1;
      wait2<C_FULL, C_PUB, 1>(L + L_CTL, wfull, job == 5 ? 16 : job == 6 ? 20 : 0, full, pub, budget);
      STAMP(2 * job + 1);
      u32x4 d[4], e;
      int npass = 0;
      for (;;) {
        if ((a.trig[7] >> (8 + job)) & 1) {
#pragma unroll
          for (int i = 0; i < 4; i++) d[i] = __builtin_amdgcn_raw_buffer_load_b128(rw, (2 * i + (lane >> 5) == skip) ? OOB : lane * 16 + i * 1024, off, 18);
        } else {
#pragma unroll
          for (int i = 0; i < 4; i++) d[i] = __builtin_amdgcn_raw_buffer_load_b128(rw, (2 * i + (lane >> 5) == skip) ? OOB : lane * 16 + i * 1024, off, 16);
        }
        e = __builtin_amdgcn_raw_buffer_load_b128(rw, eoff, 0, 16);
        u32 bad = extra ? ((e[0] ^ t8) | (e[1] ^ t8)) | ((e[2] ^ t8) | (e[3] ^ t8)) : 0u;
#pragma unroll
        for (int i = 0; i < 4; i++) bad |= (2 * i + (lane >> 5) == skip) ? 0u : ((d[i][0] ^ t8) | (d[i][1] ^ t8)) | ((d[i][2] ^ t8) | (d[i][3] ^ t8));
        npass++;
        if (__all((bad & TMASK) == 0)) break;
        if (--gbudget < 0) break;
        if (job == 5) wait2<C_FULL, C_PUB, 1>(L + L_CTL, min(wfull + 4 * npass, 4 * T_END), 0, full, pub, budget);
        asm volatile("" ::: "memory");
      }
      STAMPV(16 + job, npass);
#pragma unroll
      for (int i = 0; i < 4; i++) d[i] &= ~TMASK;
      e &= ~TMASK;
      if (job == 2 || job == 4 || job == 5) {
        const u32 img = L + (job == 2 ? L_QB : job == 4 ? L_OB : L_OA) + (lane >> 5) * RS + (lane & 31) * 8;
        if (job == 4) {
#pragma unroll
          for (int i = 0; i < 4; i++) {
            const float gt = u2f(((d[i][0] & 0xff00u) << 8) | ((d[i][1] & 0xff00u) << 16));
            d[i] &= 0xffff0000u;
            stl<f32x2>(L + L_G + (2 * i + (lane >> 5)) * 8, f32x2{rbf(1.0f - gt), gt});
          }
        }
#pragma unroll
        for (int i = 0; i < 4; i++)
          if (2 * i + (lane >> 5) != skip) stl<u32x2>(img + 2 * i * RS, u32x2{pack2(u2f(d[i][0]), u2f(d[i][1])), pack2(u2f(d[i][2]), u2f(d[i][3]))});
        waitlds();
        stctl(L + L_CTL + C_RDY, job == 2 ? 2 : job - 1);
      } else {
        float wn = 1.0f;
        if (job == 3) {
          float M = u2f(e[0]);
          M = fmaxf(M, dpp<XOR1>(M)); M = fmaxf(M, dpp<XOR2>(M)); M = fmaxf(M, dpp<XOR7>(M));
          const float wt = __builtin_amdgcn_exp2f((u2f(e[0]) - M) * 1.4426950408889634f);
          float Ls = wt * u2f(e[1]);
          Ls += dpp<XOR1>(Ls); Ls += dpp<XOR2>(Ls); Ls += dpp<XOR7>(Ls);
          wn = wt / Ls;
        }
        f32x4 s = {0.f, 0.f, 0.f, 0.f};
#pragma unroll
        for (int i = 0; i < 4; i++) {
          const float w0 = u2f(__builtin_amdgcn_readlane(f2u(wn), 2 * i)), w1 = u2f(__builtin_amdgcn_readlane(f2u(wn), 2 * i + 1));
          const float wi = lane < 32 ? w0 : w1;
#pragma unroll
          for (int k = 0; k < 4; k++) s[k] = fmaf(wi, u2f(d[i][k]), s[k]);
        }
        f32x4 o;
#pragma unroll
        for (int k = 0; k < 4; k++) { s[k] = x32_sum(s[k]); o[k] = x16_sum(s[k] + dpp<XOR8>(s[k])); }
        __builtin_amdgcn_raw_buffer_store_b64(u32x2{pack2(o[0], o[1]), pack2(o[2], o[3])}, ro, (job == 6 && lane < 8) ? (b * DM + h * 32 + lane * 4) * 2 : OOB, 0, 0);
        if (job == 6) break;
        const u32 p01 = pack2(s[0], s[1]), p23 = pack2(s[2], s[3]);
        if (job == 0) {
          stl<u32x2>(L + L_QA + (lane & 31) * 8, u32x2{p01, p23});
          waitlds();
          stctl(L + L_CTL + C_RDY, 1);
        } else {
          const u32 gb = job == 3 ? f2u(gate) : 0u;
          const u32x4 r = {(p01 << 16) | ((gb >> 8) & 0xff00u) | t8, (p01 & 0xffff0000u) | ((gb >> 16) & 0xff00u) | t8, (p23 << 16) | t8, (p23 & 0xffff0000u) | t8};
          const u32 po = job == 1 ? W_QF + cu * 512 + lane * 16 : W_OB + (h * 8 + b) * 512 + lane * 16;
          __builtin_amdgcn_raw_buffer_store_b128(r, rw, lane < 32 ? po : OOB, 0, 16);
        }
      }
      STAMP(2 * job + 2);
    }
    if (wg == 0 && lane == 0) __builtin_amdgcn_raw_buffer_store_b32(tag, rw, 0, W_E, 16);
    STAMP(14);
    STAMP_DUMP();
  }
}

struct Tune {
  int inf = 20, infh = 28;
  int pq = 3500, pq1 = 3900, pqb = 3600, pq2 = 3580, ts1 = 20, ts2 = 112;
  int th0 = 0, th1 = 4000, th = 60;
  int anc0 = -1000, setp = 35, ck0 = 48, ckstep = 64, cklag = 16, cklast = 560;
  int trig[8] = {28, 56, 160, 400, 496, 600, 0, 0};
  int blo = 4096, bstep = 32768;
};

extern "C" void kernel_launch(void* const* d_in, const int* in_sizes, int n_in, void* d_out, int out_size, void* d_ws, size_t ws_size,
                              hipStream_t stream) {
  const Tune T;
  Args a;
  a.wqa = (const char*)d_in[3];
  a.x = (const u16*)d_in[0];
  a.gw = (const u16*)d_in[10];
  a.out = (u16*)d_out;
  a.ws = (char*)d_ws;
  a.anc0 = T.anc0 * 256; a.setp = T.setp * 256; a.ck0 = T.ck0; a.ckstep = T.ckstep; a.cklag = T.cklag; a.cklast = T.cklast;
  for (int i = 0; i < 8; i++) a.trig[i] = T.trig[i];
  const char *wqb = (const char*)d_in[8], *ka = (const char*)d_in[1], *kb = (const char*)d_in[6], *vb = (const char*)d_in[7], *va = (const char*)d_in[2];
  const char *wob = (const char*)d_in[9], *woa = (const char*)d_in[4];
  const u32 KT = 0x80000000u;
  auto ts = [](int th, int tile) { return ((u32)th * 256 & ~0x3ffu) | (u32)(4 * tile); };
  auto mh = [](u32 hi, u32 lo) { return (hi >> 8 << 16) | (lo >> 8); };
  auto rw = [](u32 row, u32 step) { return row | (step >> 8 << 16); };
  const u32 bl = (u32)T.blo, bs = (u32)T.bstep, bn = 31 * bs + 4096;
  a.seg[0] = Seg{wqb, 128 * 8192, mh(2048, 256), 127 * 8192 + 256, rw(8192, 16 * 8192), ts(T.th0, T_AK), (u32)T.pq};
  a.seg[1] = Seg{ka, 32u << 20, mh(8u << 20, 1u << 20), (u32)(T.ts1 - T_AK) * TILE, rw(256, 4096) | KT, ts(T.th1, T.ts1), (u32)T.pq1};
  a.seg[2] = Seg{ka + (T.ts1 - T_AK) * TILE, 32u << 20, mh(8u << 20, 1u << 20), (u32)(T_BK - T.ts1) * TILE, rw(256, 4096) | KT, ts(T.th, T_BK), (u32)T.pq};
  a.seg[3] = Seg{kb, 4u << 20, mh(1u << 20, bl), bn, rw(256, bs) | KT, ts(T.th, T_BV), (u32)T.pqb};
  a.seg[4] = Seg{vb, 4u << 20, mh(1u << 20, bl), bn, rw(256, bs), ts(T.th, T_AV), (u32)T.pqb};
  a.seg[5] = Seg{va, 32u << 20, mh(8u << 20, 1u << 20), (u32)(T.ts2 - T_AV) * TILE, rw(256, 4096), ts(T.th, T.ts2), (u32)T.pq2};
  a.seg[6] = Seg{va + (T.ts2 - T_AV) * TILE, 32u << 20, mh(8u << 20, 1u << 20), (u32)(T_WO - T.ts2) * TILE, rw(256, 4096), ts(T.th, T_WO), (u32)T.pq};
  a.seg[7] = Seg{wob, 256, mh(8 * 128 * 2048, 128 * 2048), 127 * 2048 + 256, rw(2048, 16 * 2048), ts(T.th, T_WO + 2), (u32)T.pq};
  a.seg[8] = Seg{woa, 256, mh(8 * 128 * 2048, 128 * 2048), 127 * 2048 + 256, rw(2048, 16 * 2048), ts(T.th, T_END), (u32)T.pq};
  a.seg[9] = a.seg[8];
  fused_decode<20, 28><<<256, NWAVE * 64, 0, stream>>>(a);
}
```
